```python
import torch

def _softmax(x, axis=-1):
    return torch.softmax(x, dim=axis)
def _gelu(x):
    return torch.nn.functional.gelu(x, approximate="tanh")
def _silu(x):
    return torch.nn.functional.silu(x)
def _sigmoid(x):
    return torch.sigmoid(x)
def _layernorm(x, axis=-1, eps=1e-5):
    m = x.mean(dim=axis, keepdim=True)
    v = x.var(dim=axis, keepdim=True, unbiased=False)
    return (x - m) / torch.sqrt(v + eps)
def _scatter_add(tgt, idx, src, axis=0):
    return tgt.clone().index_add_(axis, idx, src)
def _segment_sum(src, seg, n):
    out = torch.zeros((n,) + src.shape[1:], dtype=src.dtype)
    return out.index_add_(0, seg, src)
def _pad(x, pw):
    flat = tuple(v for pair in reversed(pw) for v in pair)
    return torch.nn.functional.pad(x, flat)

kernel_name = 'standalone__gqa_softmax__near_ridge'

def setup_inputs():

    _w = (lambda a: (lambda t: t.to(torch.bfloat16) if t.is_floating_point() else t)(torch.as_tensor(a)))
    B, Hq, Hkv, Sq, Sk, D = 8, 32, 32, 768, 512, 192
    return {
        "q": _w((lambda *s: torch.randn(*s, dtype=torch.bfloat16) * 0.8)(B, Hq, Sq, D)),
        "k": _w((lambda *s: torch.randn(*s, dtype=torch.bfloat16) * 0.8)(B, Hkv, Sk, D)),
        "v": _w((lambda *s: torch.randn(*s, dtype=torch.bfloat16) * 0.05)(B, Hkv, Sk, D)),
    }

def reference(q, k, v):
    B, Hq, Sq, D = q.shape
    Hkv = k.shape[1]
    rep = Hq // Hkv
    k = (lambda x, n, axis: torch.repeat_interleave(x, n, dim=axis))(k, rep, axis=1)
    v = (lambda x, n, axis: torch.repeat_interleave(x, n, dim=axis))(v, rep, axis=1)
    scale = D ** -0.5
    scores = torch.einsum("bhqd,bhkd->bhqk", q, k) * scale
    p = _softmax(scores, axis=-1)
    return torch.einsum("bhqk,bhkd->bhqd", p, v)
```

```cpp
#include <hip/hip_runtime.h>
#include <hip/hip_bf16.h>

#define DBG 0

__device__ __forceinline__ float bf2f(unsigned short x) { return __builtin_bit_cast(float, (unsigned)x << 16); }
__device__ __forceinline__ float xlane(float v, int lane, int m) {
  return __builtin_bit_cast(float, __builtin_amdgcn_ds_bpermute((lane ^ m) << 2, __builtin_bit_cast(int, v)));
}

__global__ __launch_bounds__(256, 1) __attribute__((aligned(4096))) void attn_kernel(const char* __restrict__ q, const char* __restrict__ k, const char* __restrict__ v,
                                                      char* __restrict__ o, char* __restrict__ dbg) {
  __shared__ __attribute__((aligned(16))) char lds[163840];
  const unsigned w = __builtin_amdgcn_readfirstlane(threadIdx.x >> 6);
  const unsigned bid = blockIdx.x;
  unsigned flags;
  asm volatile(
R"(
.macro PV a,b,c
v_mfma_f32_16x16x32_bf16 v[\a:\a+3], a[\b:\b+3], v[\c:\c+3], v[\a:\a+3]
.endm
.macro PZ a,b,c
v_mfma_f32_16x16x32_bf16 v[\a:\a+3], a[\b:\b+3], v[\c:\c+3], 0
.endm
.macro PW a,b,c
v_mfma_f32_16x16x32_bf16 v[\a:\a+3], v[\b:\b+3], a[\c:\c+3], v[\a:\a+3]
.endm
.macro PX a,b,c
v_mfma_f32_16x16x32_bf16 v[\a:\a+3], v[\b:\b+3], a[\c:\c+3], 0
.endm
.macro QK a,b,c
v_mfma_f32_16x16x32_bf16 v[\a:\a+3], a[\b:\b+3], a[\c:\c+3], v[\a:\a+3]
.endm
.macro QZ a,b,c
v_mfma_f32_16x16x32_bf16 v[\a:\a+3], a[\b:\b+3], a[\c:\c+3], 0
.endm
.macro QC a,b,c
v_mfma_f32_16x16x32_bf16 v[\a:\a+3], a[\b:\b+3], a[\c:\c+3], -4.0
.endm
.macro RQ a,b,c
ds_read_b128 a[\a:\a+3], v[\b] offset:\c
.endm
.macro RT a,b,c
ds_read_b64_tr_b16 a[\a:\a+1], v[\b] offset:\c
.endm
.macro CV a,b,c
v_cvt_pk_bf16_f32 v[\a], v[\b], v[\c]
.endm
s_load_dwordx8 s[88:95], %1, 0
s_mov_b32 s86, %3
v_mbcnt_lo_u32_b32 v0, -1, 0
v_mbcnt_hi_u32_b32 v0, -1, v0
s_getpc_b64 s[78:79]
L_pc_%=:
s_lshr_b32 s73, %2, 3
s_lshl_b32 s73, s73, 2
s_add_u32 s73, s73, %3
s_lshl_b32 s73, s73, 10
v_lshl_add_u32 v1, v0, 4, s73
v_min_u32 v1, L_end_%=-L_pc_%=-4, v1
global_load_dword v10, v1, s[78:79] 
s_and_b32 s74, %2, 7
s_lshr_b32 s81, %2, 3
s_add_u32 s87, s81, 60
s_lshl_b32 s76, s81, 1
s_sub_u32 s76, s76, 61
s_cmp_lt_u32 s81, 30
s_cselect_b32 s81, s81, s87
s_cselect_b32 s75, 30, 3
s_cselect_b32 s76, 30, s76
s_mul_i32 s60, s86, 3072
s_lshl_b32 s73, s60, 3
s_mul_i32 s87, s81, 171
s_lshr_b32 s87, s87, 9
s_mul_i32 s77, s87, 21
s_add_u32 s77, s77, s81
s_lshl_b32 s87, s87, 3
s_add_u32 s87, s87, s74
s_mul_i32 s62, s87, 196608
s_add_u32 s62, s62, s60
s_mul_i32 s87, s74, 3
s_add_u32 s77, s77, s87
s_mul_i32 s68, s77, 98304
s_add_u32 s68, s68, s73
s_add_u32 s81, s81, s75
v_lshrrev_b32 v1, 3, v0
v_and_b32 v2, 6, v1
v_and_b32 v3, 7, v0
v_xor_b32 v2, v2, v3
v_lshlrev_b32 v2, 4, v2
v_mul_u32_u24 v3, 384, v1
v_add_u32 v237, v2, v3
s_lshl_b32 s61, s86, 14
s_add_u32 s61, s61, 98304
s_sub_u32 s56, s62, 6144
s_cmp_ge_u32 s86, 2
s_cselect_b32 s56, s56, 1073741824
s_mov_b32 s46, 50331648
s_mov_b32 s47, 0x20000
s_mov_b32 s42, 75497472
s_mov_b32 s43, 0x20000
s_mul_i32 s87, s81, 171
s_lshr_b32 s87, s87, 9
s_mul_i32 s77, s87, 21
s_add_u32 s77, s77, s81
s_lshl_b32 s87, s87, 3
s_add_u32 s87, s87, s74
s_mul_i32 s63, s87, 196608
s_add_u32 s63, s63, s60
s_mul_i32 s87, s74, 3
s_add_u32 s77, s77, s87
s_mul_i32 s69, s77, 98304
s_add_u32 s69, s69, s73
s_add_u32 s81, s81, s76
s_mul_i32 s87, s81, 171
s_lshr_b32 s87, s87, 9
s_mul_i32 s77, s87, 21
s_add_u32 s77, s77, s81
s_lshl_b32 s87, s87, 3
s_add_u32 s87, s87, s74
s_mul_i32 s64, s87, 196608
s_add_u32 s64, s64, s60
s_mul_i32 s87, s74, 3
s_add_u32 s77, s77, s87
s_mul_i32 s70, s77, 98304
s_add_u32 s70, s70, s73
s_sub_u32 s87, s63, 6144
s_sub_u32 s77, s64, 6144
s_add_u32 s65, s62, 190464
s_add_u32 s66, s63, 190464
s_add_u32 s67, s64, 190464
s_cmp_ge_u32 s86, 2
s_cselect_b32 s65, s87, s65
s_cselect_b32 s66, s77, s66
s_cselect_b32 s67, 1073741824, s67
s_mov_b32 s50, 50331648
s_mov_b32 s51, 0x20000
s_mov_b32 s54, 75497472
s_mov_b32 s55, 0x20000
s_mov_b32 s71, 0x3dd53b95
s_mov_b32 s72, 0
s_mov_b32 s74, 0x21800000
s_mov_b32 s75, 0x53800000
v_and_b32 v4, 15, v0
s_waitcnt lgkmcnt(0)
s_mov_b32 s44, s90
s_and_b32 s45, s91, 0xffff
s_add_u32 m0, s60, 0
s_nop 0
buffer_load_dwordx4 v237, s[44:47], s56 offen offset:0 nt lds
s_mov_b32 s40, s88
s_and_b32 s41, s89, 0xffff
s_add_u32 s58, s68, 0
s_add_u32 m0, s61, 0
s_nop 0
buffer_load_dwordx4 v237, s[40:43], s58 offen offset:0 nt lds
s_load_dword s96, s[92:93], s62
v_lshrrev_b32 v5, 4, v0
v_lshl_add_u32 v3, v5, 6, s61
v_lshl_add_u32 v246, v0, 4, v3
v_bfe_u32 v3, v0, 5, 1
v_mul_u32_u24 v3, 640, v3
v_add_u32 v247, s61, v3
v_bfe_u32 v3, v0, 4, 1
v_lshl_add_u32 v247, v3, 3, v247
v_bfe_u32 v3, v0, 0, 2
v_lshl_add_u32 v247, v3, 6, v247
v_bfe_u32 v3, v0, 2, 2
v_lshl_add_u32 v247, v3, 4, v247
s_add_u32 m0, s60, 896
s_nop 0
buffer_load_dwordx4 v237, s[44:47], s56 offen offset:128 nt lds
v_bfe_u32 v3, v0, 5, 1
v_mul_u32_u24 v3, 3072, v3
v_add_u32 v242, 0, v3
v_bfe_u32 v3, v0, 4, 1
v_mul_u32_u24 v3, 384, v3
v_add_u32 v242, v242, v3
v_bfe_u32 v3, v0, 0, 2
v_lshl_add_u32 v242, v3, 4, v242
v_bfe_u32 v3, v0, 2, 1
v_lshl_add_u32 v242, v3, 3, v242
v_bfe_u32 v3, v0, 3, 1
v_lshl_add_u32 v242, v3, 6, v242
s_add_u32 m0, s60, 1792
s_nop 0
buffer_load_dwordx4 v237, s[44:47], s56 offen offset:256 nt lds
v_bfe_u32 v3, v0, 4, 2
v_mul_u32_u24 v3, 320, v3
v_add_u32 v6, 0, v3
v_bfe_u32 v3, v0, 1, 1
v_lshl_add_u32 v6, v3, 3, v6
v_bfe_u32 v3, v0, 0, 1
v_lshl_add_u32 v6, v3, 7, v6
v_bfe_u32 v3, v0, 2, 2
v_lshl_add_u32 v6, v3, 4, v6
v_bfe_u32 v3, v0, 4, 2
v_mul_u32_u24 v3, 1536, v3
v_add_u32 v243, 0, v3
v_bfe_u32 v3, v0, 3, 1
v_mul_u32_u24 v3, 384, v3
v_add_u32 v243, v243, v3
v_bfe_u32 v3, v0, 0, 3
v_lshl_add_u32 v243, v3, 4, v243
v_lshl_add_u32 v243, v6, 16, v243
s_lshl_b32 s80, s86, 8
s_add_u32 s73, s80, 101376
s_add_u32 s80, s80, 36096
v_lshl_add_u32 v254, v5, 4, s73
v_and_b32 v3, 6, v4
v_xor_b32 v3, v3, v5
s_add_u32 m0, s61, -2048
s_nop 0
buffer_load_dwordx4 v237, s[40:43], s58 offen offset:3072 nt lds
v_lshlrev_b32 v3, 4, v3
v_and_b32 v6, 7, v4
v_lshl_add_u32 v3, v6, 7, v3
v_lshrrev_b32 v6, 3, v4
v_mul_u32_u24 v7, 3072, v6
v_add_u32 v238, v3, v7
v_xor_b32 v239, 64, v238
v_lshl_add_u32 v3, v6, 10, v3
v_add_u32 v244, s61, v3
v_xor_b32 v245, 64, v244
v_bfe_u32 v3, v0, 2, 3
v_and_b32 v6, 6, v3
s_add_u32 s58, s68, 0
s_add_u32 m0, s61, 1920
s_nop 0
buffer_load_dwordx4 v237, s[40:43], s58 offen offset:128 nt lds
v_and_b32 v7, 2, v0
v_lshlrev_b32 v7, 1, v7
v_xor_b32 v6, v6, v7
v_and_b32 v7, 1, v0
v_lshl_add_u32 v6, v6, 1, v7
v_lshlrev_b32 v6, 3, v6
v_lshl_add_u32 v6, v3, 7, v6
v_lshrrev_b32 v7, 5, v0
v_mul_u32_u24 v8, 3072, v7
v_add_u32 v6, v6, v8
v_add_u32 v240, 49152, v6
v_xor_b32 v241, 32, v240
s_add_u32 m0, s61, -128
s_nop 0
buffer_load_dwordx4 v237, s[40:43], s58 offen offset:3200 nt lds
v_lshrrev_b32 v6, 1, v4
v_lshlrev_b32 v7, 1, v7
v_sub_u32 v6, v6, v7
v_mov_b32 v3, 0x3f803f80
v_cmp_eq_u32 vcc, 0, v6
v_cndmask_b32 v8, 0, v3, vcc
v_accvgpr_write_b32 a236, v8
v_accvgpr_write_b32 a237, v8
v_accvgpr_write_b32 a238, v8
v_accvgpr_write_b32 a239, v8
v_cmp_eq_u32 vcc, 4, v6
v_cndmask_b32 v8, 0, v3, vcc
s_add_u32 s58, s68, 0
s_add_u32 m0, s61, 3840
s_nop 0
buffer_load_dwordx4 v237, s[40:43], s58 offen offset:256 nt lds
v_accvgpr_write_b32 a240, v8
v_accvgpr_write_b32 a241, v8
v_accvgpr_write_b32 a242, v8
v_accvgpr_write_b32 a243, v8
v_cmp_eq_u32 vcc, 12, v4
v_mov_b32 v192, 0
v_mov_b32 v193, 0
v_mov_b32 v194, 0
v_mov_b32 v195, 0
v_cndmask_b32 v248, 0, v3, vcc
v_cndmask_b32 v249, 0, v3, vcc
v_cndmask_b32 v250, 0, v3, vcc
s_add_u32 m0, s61, 1792
s_nop 0
buffer_load_dwordx4 v237, s[40:43], s58 offen offset:3328 nt lds
v_cndmask_b32 v251, 0, v3, vcc
v_and_b32 v3, 1, v0
v_mul_u32_u24 v3, 0xaaaa, v3
v_add_u32 v3, 0x4444, v3
v_lshl_or_b32 v236, v3, 16, v3
v_accvgpr_write_b32 a232, 0
v_accvgpr_write_b32 a233, 0
v_accvgpr_write_b32 a234, 0
v_accvgpr_write_b32 a235, 0
s_add_u32 s58, s68, 6144
s_add_u32 m0, s61, 6144
s_nop 0
buffer_load_dwordx4 v237, s[40:43], s58 offen offset:0 nt lds
s_add_u32 m0, s61, 4096
s_nop 0
buffer_load_dwordx4 v237, s[40:43], s58 offen offset:3072 nt lds
s_mov_b32 s48, s92
s_and_b32 s49, s93, 0xffff
s_mov_b32 s52, s94
s_and_b32 s53, s95, 0xffff
s_waitcnt vmcnt(11)
s_mov_b64 exec, 0
s_cmp_eq_u32 s86, 0
s_cbranch_scc1 L_c1_%=
s_cmp_eq_u32 s86, 1
s_cbranch_scc1 L_c2_%=
s_cmp_eq_u32 s86, 2
s_cbranch_scc1 L_c3_%=
s_branch L_c4_%=
L_ret_%=:
s_mov_b64 exec, -1
s_waitcnt vmcnt(7)
s_barrier
RQ 96,238,6144
RQ 100,239,6144
RQ 104,238,7168
RQ 108,239,7168
RQ 112,238,8192
RQ 116,239,8192
s_waitcnt lgkmcnt(0)
QZ 16,196,200
QZ 32,200,204
QZ 48,204,208
QZ 64,208,212
QZ 80,212,192
QZ 0,192,200
QZ 16,196,204
QZ 32,200,208
QZ 48,204,212
QZ 64,208,192
s_waitcnt vmcnt(6)
RQ 0,244,0
RQ 4,245,0
s_add_u32 s58, s68, 6144
s_add_u32 m0, s61, 8064
s_nop 0
buffer_load_dwordx4 v237, s[40:43], s58 offen offset:128 nt lds
s_add_u32 m0, s61, 6016
s_nop 0
buffer_load_dwordx4 v237, s[40:43], s58 offen offset:3200 nt lds
QZ 80,212,196
QZ 0,192,204
QZ 16,196,208
QZ 32,200,212
QZ 48,204,192
QZ 64,208,196
QZ 80,212,200
QZ 0,192,208
QZ 16,196,212
QZ 32,200,192
QZ 48,204,196
QZ 64,208,200
QZ 80,212,204
QZ 0,192,212
QZ 16,196,192
QZ 32,200,196
s_waitcnt vmcnt(6)
RQ 8,244,2048
RQ 12,245,2048
s_add_u32 s58, s68, 6144
s_add_u32 m0, s61, 9984
s_nop 0
buffer_load_dwordx4 v237, s[40:43], s58 offen offset:256 nt lds
s_add_u32 m0, s61, 7936
s_nop 0
buffer_load_dwordx4 v237, s[40:43], s58 offen offset:3328 nt lds
QZ 48,204,200
QZ 64,208,204
QZ 80,212,208
QZ 0,192,192
QZ 16,196,196
QZ 32,200,200
QZ 48,204,204
QZ 64,208,208
QZ 80,212,212
QZ 0,192,196
QZ 16,196,200
QZ 32,200,204
QZ 48,204,208
QZ 64,208,212
QZ 80,212,192
QZ 0,192,200
s_waitcnt vmcnt(6)
RQ 16,244,4096
RQ 20,245,4096
s_add_u32 s56, s62, 6144
s_add_u32 m0, s60, 12288
s_nop 0
buffer_load_dwordx4 v237, s[44:47], s56 offen offset:0 nt lds
s_add_u32 m0, s60, 13184
s_nop 0
buffer_load_dwordx4 v237, s[44:47], s56 offen offset:128 nt lds
s_add_u32 m0, s60, 14080
s_nop 0
buffer_load_dwordx4 v237, s[44:47], s56 offen offset:256 nt lds
s_waitcnt lgkmcnt(5)
QC 228,96,0
s_waitcnt lgkmcnt(4)
QK 228,100,4
s_waitcnt lgkmcnt(3)
QK 228,104,8
s_waitcnt lgkmcnt(2)
QK 228,108,12
s_waitcnt lgkmcnt(1)
QK 228,112,16
s_waitcnt lgkmcnt(0)
QK 228,116,20
s_nop 7
v_mul_f32 v228, s71, v228
v_mul_f32 v229, s71, v229
v_mul_f32 v230, s71, v230
v_mul_f32 v231, s71, v231
v_exp_f32 v228, v228
v_exp_f32 v229, v229
v_exp_f32 v230, v230
v_exp_f32 v231, v231
s_nop 0
CV 180,228,229
CV 181,230,231
QZ 16,196,204
QZ 32,200,208
QZ 48,204,212
QZ 64,208,192
QZ 80,212,196
QZ 0,192,204
s_waitcnt vmcnt(7)
)"
R"(
RQ 24,244,6144
RQ 28,245,6144
s_add_u32 s58, s68, 12288
s_add_u32 m0, s61, 12288
s_nop 0
buffer_load_dwordx4 v237, s[40:43], s58 offen offset:0 nt lds
s_add_u32 m0, s61, 10240
s_nop 0
buffer_load_dwordx4 v237, s[40:43], s58 offen offset:3072 nt lds
QZ 16,196,208
QZ 32,200,212
QZ 48,204,192
QZ 64,208,196
QZ 80,212,200
QZ 0,192,208
QZ 16,196,212
QZ 32,200,192
QZ 48,204,196
QZ 64,208,200
QZ 80,212,204
QZ 0,192,212
s_waitcnt vmcnt(7)
RQ 32,244,8192
RQ 36,245,8192
s_add_u32 s58, s68, 12288
s_add_u32 m0, s61, 14208
s_nop 0
buffer_load_dwordx4 v237, s[40:43], s58 offen offset:128 nt lds
s_add_u32 m0, s61, 12160
s_nop 0
buffer_load_dwordx4 v237, s[40:43], s58 offen offset:3200 nt lds
QZ 16,196,192
QZ 32,200,196
QZ 48,204,200
QZ 64,208,204
QZ 80,212,208
QZ 0,192,192
QZ 16,196,196
QZ 32,200,200
QZ 48,204,204
QZ 64,208,208
QZ 80,212,212
QZ 0,192,196
s_waitcnt vmcnt(7)
RQ 40,244,10240
RQ 44,245,10240
s_add_u32 s58, s68, 12288
s_add_u32 m0, s61, -256
s_nop 0
buffer_load_dwordx4 v237, s[40:43], s58 offen offset:256 nt lds
s_add_u32 m0, s61, -2304
s_nop 0
buffer_load_dwordx4 v237, s[40:43], s58 offen offset:3328 nt lds
s_waitcnt lgkmcnt(5)
QC 108,96,24
s_waitcnt lgkmcnt(4)
QK 108,100,28
s_waitcnt lgkmcnt(3)
QK 108,104,32
s_waitcnt lgkmcnt(2)
QK 108,108,36
s_waitcnt lgkmcnt(1)
QK 108,112,40
s_waitcnt lgkmcnt(0)
QK 108,116,44
s_nop 7
v_mul_f32 v108, s71, v108
v_mul_f32 v109, s71, v109
v_mul_f32 v110, s71, v110
v_mul_f32 v111, s71, v111
v_exp_f32 v108, v108
v_exp_f32 v109, v109
v_exp_f32 v110, v110
v_exp_f32 v111, v111
s_nop 0
CV 184,108,109
CV 185,110,111
QZ 16,196,200
QZ 32,200,204
QZ 48,204,208
QZ 64,208,212
s_waitcnt vmcnt(6)
s_barrier
RQ 120,238,12288
RQ 124,239,12288
RQ 128,238,13312
RQ 132,239,13312
RQ 136,238,14336
RQ 140,239,14336
RQ 144,238,18432
RQ 148,239,18432
RQ 152,238,19456
RQ 156,239,19456
RQ 160,238,20480
RQ 164,239,20480
s_add_u32 s57, s62, 0
s_add_u32 m0, s60, 73728
s_nop 0
buffer_load_dwordx4 v237, s[48:51], s57 offen offset:0 nt lds
s_add_u32 m0, s60, 74624
s_nop 0
buffer_load_dwordx4 v237, s[48:51], s57 offen offset:128 nt lds
s_add_u32 m0, s60, 75520
s_nop 0
buffer_load_dwordx4 v237, s[48:51], s57 offen offset:256 nt lds
s_waitcnt lgkmcnt(11)
QC 112,120,0
s_waitcnt lgkmcnt(10)
QK 112,124,4
s_waitcnt lgkmcnt(9)
QK 112,128,8
s_waitcnt lgkmcnt(8)
QK 112,132,12
s_waitcnt lgkmcnt(7)
QK 112,136,16
s_waitcnt lgkmcnt(6)
QK 112,140,20
s_nop 0
QC 228,120,24
s_nop 0
QK 228,124,28
s_nop 0
QK 228,128,32
s_nop 0
QK 228,132,36
v_mul_f32 v113, s71, v113
v_mul_f32 v115, s71, v115
QK 228,136,40
v_mul_f32 v112, s71, v112
v_mul_f32 v114, s71, v114
QK 228,140,44
s_waitcnt lgkmcnt(5)
v_exp_f32 v114, v114
QC 108,144,0
s_waitcnt lgkmcnt(4)
v_exp_f32 v115, v115
QK 108,148,4
s_waitcnt lgkmcnt(3)
v_exp_f32 v112, v112
QK 108,152,8
s_waitcnt lgkmcnt(2)
v_exp_f32 v113, v113
CV 183,114,115
QK 108,156,12
s_waitcnt lgkmcnt(1)
v_mul_f32 v229, s71, v229
v_mul_f32 v230, s71, v230
QK 108,160,16
s_waitcnt lgkmcnt(0)
CV 182,112,113
v_mul_f32 v231, s71, v231
QK 108,164,20
v_mul_f32 v228, s71, v228
v_exp_f32 v229, v229
QC 112,144,24
v_exp_f32 v228, v228
QK 112,148,28
v_exp_f32 v230, v230
QK 112,152,32
v_exp_f32 v231, v231
QK 112,156,36
v_mul_f32 v108, s71, v108
v_mul_f32 v110, s71, v110
QK 112,160,40
CV 186,228,229
v_mul_f32 v111, s71, v111
v_exp_f32 v108, v108
v_exp_f32 v110, v110
v_exp_f32 v111, v111
QK 112,164,44
CV 187,230,231
v_mul_f32 v109, s71, v109
v_exp_f32 v109, v109
s_nop 0
CV 196,108,109
CV 197,110,111
s_nop 1
v_mul_f32 v112, s71, v112
v_mul_f32 v113, s71, v113
v_mul_f32 v114, s71, v114
v_mul_f32 v115, s71, v115
v_exp_f32 v112, v112
v_exp_f32 v113, v113
v_exp_f32 v114, v114
v_exp_f32 v115, v115
CV 200,112,113
CV 201,114,115
s_waitcnt vmcnt(7)
RQ 48,244,12288
RQ 52,245,12288
s_add_u32 s56, s62, 18432
s_add_u32 m0, s60, 24576
s_nop 0
buffer_load_dwordx4 v237, s[44:47], s56 offen offset:0 nt lds
s_add_u32 m0, s60, 25472
s_nop 0
buffer_load_dwordx4 v237, s[44:47], s56 offen offset:128 nt lds
s_add_u32 m0, s60, 26368
s_nop 0
buffer_load_dwordx4 v237, s[44:47], s56 offen offset:256 nt lds
s_waitcnt lgkmcnt(1)
QC 228,96,48
s_waitcnt lgkmcnt(0)
QK 228,100,52
QC 108,120,48
QK 108,124,52
QC 112,144,48
QK 112,148,52
s_waitcnt vmcnt(8)
RQ 56,244,14336
RQ 60,245,14336
s_add_u32 s77, s62, 12288
s_add_u32 m0, s60, 86016
s_nop 0
buffer_load_dwordx4 v237, s[48:51], s77 offen offset:0 nt lds
s_add_u32 m0, s60, 86912
s_nop 0
buffer_load_dwordx4 v237, s[48:51], s77 offen offset:128 nt lds
s_add_u32 m0, s60, 87808
s_nop 0
buffer_load_dwordx4 v237, s[48:51], s77 offen offset:256 nt lds
s_waitcnt lgkmcnt(1)
QK 228,104,56
s_waitcnt lgkmcnt(0)
QK 228,108,60
QK 108,128,56
QK 108,132,60
QK 112,152,56
QK 112,156,60
s_waitcnt vmcnt(9)
RQ 64,244,0
RQ 68,245,0
s_add_u32 s56, s62, 30720
s_add_u32 m0, s60, 36864
s_nop 0
buffer_load_dwordx4 v237, s[44:47], s56 offen offset:0 nt lds
s_add_u32 m0, s60, 37760
s_nop 0
buffer_load_dwordx4 v237, s[44:47], s56 offen offset:128 nt lds
s_add_u32 m0, s60, 38656
s_nop 0
buffer_load_dwordx4 v237, s[44:47], s56 offen offset:256 nt lds
s_waitcnt lgkmcnt(1)
QK 228,112,64
s_waitcnt lgkmcnt(0)
QK 228,116,68
QK 108,136,64
QK 108,140,68
QK 112,160,64
QK 112,164,68
s_nop 3
v_mul_f32 v228, s71, v228
v_mul_f32 v229, s71, v229
v_mul_f32 v230, s71, v230
v_mul_f32 v231, s71, v231
v_exp_f32 v228, v228
v_exp_f32 v229, v229
v_exp_f32 v230, v230
v_exp_f32 v231, v231
s_nop 0
CV 188,228,229
CV 189,230,231
v_mul_f32 v108, s71, v108
v_mul_f32 v109, s71, v109
v_mul_f32 v110, s71, v110
v_mul_f32 v111, s71, v111
v_exp_f32 v108, v108
v_exp_f32 v109, v109
v_exp_f32 v110, v110
v_exp_f32 v111, v111
s_nop 0
CV 190,108,109
CV 191,110,111
s_waitcnt vmcnt(9)
s_barrier
RT 216,240,24576
RT 218,240,30720
RT 220,240,24592
RT 222,240,30736
s_add_u32 s58, s68, 18432
s_add_u32 m0, s61, 2048
L_c1_%=:
s_cbranch_execz L_c5_%=
s_nop 0
buffer_load_dwordx4 v237, s[40:43], s58 offen offset:0 nt lds
s_add_u32 m0, s61, 0
s_nop 0
buffer_load_dwordx4 v237, s[40:43], s58 offen offset:3072 nt lds
v_mul_f32 v112, s71, v112
v_mul_f32 v113, s71, v113
v_mul_f32 v114, s71, v114
v_mul_f32 v115, s71, v115
v_exp_f32 v112, v112
v_exp_f32 v113, v113
v_exp_f32 v114, v114
v_exp_f32 v115, v115
s_nop 0
CV 204,112,113
CV 205,114,115
v_smfmac_f32_16x16x64_bf16 a[232:235], a[236:239], v[180:187], v236
s_waitcnt lgkmcnt(2)
RT 226,241,30720
PX 0,180,216
RT 224,241,24576
PX 4,184,216
RT 230,241,30736
PX 8,188,216
s_waitcnt lgkmcnt(3)
RT 228,241,24592
PX 16,180,220
RT 218,240,31744
PX 20,184,220
RT 216,240,25600
PX 24,188,220
s_nop 3
v_smfmac_f32_16x16x64_bf16 a[232:235], a[240:243], v[188:195], v236
s_waitcnt lgkmcnt(4)
PX 32,180,224
RT 220,240,25616
PX 36,184,224
PX 40,188,224
s_waitcnt lgkmcnt(3)
RT 222,240,31760
s_nop 3
PX 48,180,228
RT 226,241,31744
PX 52,184,228
RT 224,241,25600
PX 56,188,228
s_waitcnt lgkmcnt(4)
RT 230,241,31760
PX 64,180,216
s_nop 3
PX 68,184,216
RT 228,241,25616
PX 72,188,216
s_waitcnt lgkmcnt(4)
PX 80,180,220
RT 216,240,26624
PX 84,184,220
RT 218,240,32768
s_nop 3
PX 88,188,220
s_waitcnt vmcnt(8) lgkmcnt(4)
s_add_u32 s58, s68, 18432
s_add_u32 m0, s61, 3968
RT 220,240,26640
PX 96,180,224
s_barrier
RQ 168,238,24576
RQ 172,239,24576
PX 100,184,224
RQ 176,238,25600
PX 104,188,224
s_waitcnt lgkmcnt(6)
RQ 180,239,25600
s_nop 3
v_mfma_f32_16x16x32_bf16 a[244:247], v[180:183], a[228:231], 0
RQ 184,238,26624
v_mfma_f32_16x16x32_bf16 a[248:251], v[184:187], a[228:231], 0
RQ 188,239,26624
v_mfma_f32_16x16x32_bf16 a[252:255], v[188:191], a[228:231], 0
s_waitcnt lgkmcnt(5)
buffer_load_dwordx4 v237, s[40:43], s58 offen offset:128 nt lds
QC 228,168,0
s_waitcnt lgkmcnt(4)
RT 222,240,32784
s_nop 3
QK 228,172,4
s_waitcnt lgkmcnt(4)
QK 228,176,8
s_waitcnt lgkmcnt(3)
s_add_u32 m0, s61, 1920
RT 230,241,32784
QK 228,180,12
s_waitcnt lgkmcnt(3)
ds_read_b128 v[76:79], v238 offset:32768
QK 228,184,16
s_waitcnt lgkmcnt(3)
buffer_load_dwordx4 v237, s[40:43], s58 offen offset:3200 nt lds
s_nop 3
QK 228,188,20
RT 224,241,26624
PX 116,180,216
PX 120,184,216
RT 228,241,26640
PX 124,188,216
s_waitcnt lgkmcnt(4)
RT 226,241,32768
s_nop 3
PX 132,180,220
ds_read_b128 v[12:15], v238 offset:30720
v_mul_f32 v228, s71, v228
PX 136,184,220
ds_read_b128 v[44:47], v238 offset:31744
PX 140,188,220
v_mul_f32 v229, s71, v229
QC 108,168,24
v_mul_f32 v230, s71, v230
v_mul_f32 v231, s71, v231
)"
R"(
s_nop 3
QK 108,172,28
ds_read_b128 v[60:63], v239 offset:31744
QK 108,176,32
v_exp_f32 v231, v231
QK 108,180,36
v_exp_f32 v230, v230
QK 108,184,40
s_nop 3
QK 108,188,44
s_waitcnt lgkmcnt(3)
ds_read_b128 v[28:31], v239 offset:30720
CV 199,230,231
PX 148,180,224
v_exp_f32 v228, v228
PX 152,184,224
v_exp_f32 v229, v229
PX 156,188,224
s_waitcnt vmcnt(7)
ds_read_b128 v[92:95], v239 offset:32768
CV 198,228,229
s_nop 3
PX 164,180,228
s_barrier
PX 168,184,228
RT 222,240,43024
v_mul_f32 v108, s71, v108
PX 172,188,228
s_add_u32 m0, s61, 5888
RT 226,241,43008
v_mul_f32 v110, s71, v110
QC 112,168,48
RT 218,240,43008
v_mul_f32 v109, s71, v109
s_nop 3
QK 112,172,52
s_add_u32 s58, s68, 18432
RT 220,240,36880
v_mul_f32 v111, s71, v111
QK 112,176,56
RT 216,240,36864
QK 112,180,60
buffer_load_dwordx4 v237, s[40:43], s58 offen offset:256 nt lds
QK 112,184,64
v_exp_f32 v108, v108
s_nop 3
QK 112,188,68
s_waitcnt lgkmcnt(9)
v_exp_f32 v109, v109
v_mfma_f32_16x16x32_bf16 v[228:231], v[12:15], a[0:3], -4.0
s_waitcnt lgkmcnt(6)
s_add_u32 m0, s61, 3840
v_exp_f32 v110, v110
PW 228,28,4
buffer_load_dwordx4 v237, s[40:43], s58 offen offset:3328 nt lds
PW 228,44,8
v_exp_f32 v111, v111
s_nop 3
PW 228,60,12
CV 203,110,111
v_mul_f32 v114, s71, v114
PW 228,76,16
s_waitcnt lgkmcnt(5)
v_exp_f32 v114, v114
PW 228,92,20
s_waitcnt lgkmcnt(0)
CV 202,108,109
v_mul_f32 v113, s71, v113
v_exp_f32 v113, v113
PW 0,196,216
v_mul_f32 v112, s71, v112
v_mul_f32 v115, s71, v115
s_nop 3
PW 4,200,216
v_exp_f32 v112, v112
PW 16,196,220
v_exp_f32 v115, v115
PW 20,200,220
CV 206,112,113
CV 207,114,115
v_smfmac_f32_16x16x64_bf16 a[232:235], a[236:239], v[196:203], v236
RT 224,241,36864
v_mul_f32 v230, s71, v230
s_nop 3
PW 8,204,216
PW 24,204,220
RT 220,240,37904
v_mul_f32 v231, s71, v231
v_mfma_f32_16x16x32_bf16 v[108:111], v[12:15], a[24:27], -4.0
v_exp_f32 v231, v231
PW 108,28,28
RT 228,241,36880
v_mul_f32 v229, s71, v229
s_nop 3
PW 108,44,32
RT 230,241,43024
v_mul_f32 v228, s71, v228
PW 108,60,36
v_exp_f32 v229, v229
PW 108,76,40
v_exp_f32 v228, v228
PW 108,92,44
s_waitcnt lgkmcnt(3)
v_exp_f32 v230, v230
s_nop 3
PW 32,196,224
RT 216,240,37888
PW 36,200,224
s_waitcnt vmcnt(6)
s_add_u32 s57, s62, 24576
RT 218,240,44032
PW 40,204,224
s_waitcnt lgkmcnt(2)
s_add_u32 m0, s60, 49152
s_barrier
PW 48,196,228
RQ 208,238,38912
v_mul_f32 v110, s71, v110
s_nop 3
PW 52,200,228
buffer_load_dwordx4 v237, s[48:51], s57 offen offset:0 nt lds
PW 56,204,228
s_add_u32 m0, s60, 50048
RQ 212,239,38912
v_mul_f32 v111, s71, v111
v_mfma_f32_16x16x32_bf16 v[112:115], v[12:15], a[48:51], -4.0
RT 222,240,44048
v_mul_f32 v109, s71, v109
PW 112,28,52
RQ 196,239,36864
s_nop 3
PW 112,44,56
buffer_load_dwordx4 v237, s[48:51], s57 offen offset:128 nt lds
PW 112,60,60
RQ 192,238,36864
PW 112,76,64
RQ 200,238,37888
v_mul_f32 v108, s71, v108
PW 112,92,68
s_waitcnt lgkmcnt(6)
s_add_u32 m0, s60, 50944
RQ 204,239,37888
CV 212,228,229
s_nop 3
PW 64,196,216
buffer_load_dwordx4 v237, s[48:51], s57 offen offset:256 nt lds
PW 68,200,216
RT 228,241,37904
CV 213,230,231
PW 72,204,216
s_waitcnt lgkmcnt(5)
v_exp_f32 v110, v110
PW 80,196,220
RT 230,241,44048
v_mul_f32 v112, s71, v112
s_nop 3
PW 84,200,220
RT 226,241,44032
v_mul_f32 v113, s71, v113
PW 88,204,220
s_waitcnt lgkmcnt(5)
RT 224,241,37888
QC 228,192,0
v_exp_f32 v109, v109
QK 228,196,4
s_waitcnt lgkmcnt(5)
v_exp_f32 v108, v108
s_nop 3
QK 228,200,8
s_waitcnt lgkmcnt(4)
RT 218,240,45056
CV 216,108,109
QK 228,204,12
v_mul_f32 v114, s71, v114
v_mul_f32 v115, s71, v115
s_nop 3
QK 228,208,16
v_exp_f32 v113, v113
QK 228,212,20
s_waitcnt lgkmcnt(1)
RT 220,240,38928
PW 96,196,224
v_exp_f32 v114, v114
PW 100,200,224
v_exp_f32 v111, v111
PW 104,204,224
RT 216,240,38912
CV 217,110,111
v_mfma_f32_16x16x32_bf16 a[244:247], v[196:199], a[228:231], a[244:247]
s_nop 3
v_mfma_f32_16x16x32_bf16 a[248:251], v[200:203], a[228:231], a[248:251]
RT 222,240,45072
v_mfma_f32_16x16x32_bf16 a[252:255], v[204:207], a[228:231], a[252:255]
RT 228,241,38928
v_mul_f32 v231, s71, v231
QC 108,192,24
v_exp_f32 v115, v115
QK 108,196,28
v_exp_f32 v231, v231
QK 108,200,32
v_exp_f32 v112, v112
QK 108,204,36
v_mul_f32 v229, s71, v229
v_mul_f32 v230, s71, v230
s_nop 3
QK 108,208,40
RT 224,241,38912
CV 220,112,113
QK 108,212,44
s_waitcnt lgkmcnt(3)
RT 226,241,45056
v_mul_f32 v228, s71, v228
PW 116,196,216
v_exp_f32 v230, v230
PW 120,200,216
s_waitcnt vmcnt(7)
v_exp_f32 v229, v229
PW 124,204,216
s_waitcnt lgkmcnt(3)
RT 230,241,45072
CV 221,114,115
PW 132,196,220
RQ 76,245,2048
s_nop 3
PW 136,200,220
s_add_u32 m0, s60, 0
RQ 72,244,2048
v_mul_f32 v111, s71, v111
PW 140,204,220
CV 215,230,231
v_mul_f32 v108, s71, v108
QC 112,192,48
v_mul_f32 v109, s71, v109
v_mul_f32 v110, s71, v110
QK 112,196,52
s_add_u32 s56, s62, 43008
v_exp_f32 v111, v111
QK 112,200,56
buffer_load_dwordx4 v237, s[44:47], s56 offen offset:0 nt lds
QK 112,204,60
s_add_u32 m0, s60, 896
v_exp_f32 v228, v228
s_nop 3
QK 112,208,64
buffer_load_dwordx4 v237, s[44:47], s56 offen offset:128 nt lds
QK 112,212,68
s_waitcnt lgkmcnt(3)
s_add_u32 m0, s60, 1792
v_exp_f32 v110, v110
PW 148,196,224
buffer_load_dwordx4 v237, s[44:47], s56 offen offset:256 nt lds
PW 152,200,224
s_waitcnt lgkmcnt(0)
v_exp_f32 v109, v109
QC 128,96,72
RT 96,240,24576
CV 214,228,229
QK 128,100,76
s_waitcnt vmcnt(8)
s_add_u32 m0, s60, 61440
RQ 84,245,4096
s_nop 3
QC 144,120,72
s_add_u32 s77, s62, 36864
buffer_load_dwordx4 v237, s[48:51], s77 offen offset:0 nt lds
QK 144,124,76
s_add_u32 m0, s60, 62336
RQ 80,244,4096
v_mul_f32 v112, s71, v112
QC 160,144,72
RT 100,240,24592
v_exp_f32 v108, v108
QK 160,148,76
buffer_load_dwordx4 v237, s[48:51], s77 offen offset:128 nt lds
QC 176,168,72
RT 150,240,31760
RT 170,241,31744
QK 176,172,76
RT 126,241,30736
v_mul_f32 v115, s71, v115
s_nop 3
v_mfma_f32_16x16x32_bf16 v[228:231], v[12:15], a[72:75], -4.0
RT 102,240,30736
v_exp_f32 v115, v115
PW 228,28,76
RT 98,240,30720
v_mul_f32 v113, s71, v113
QC 232,192,72
RT 144,240,25600
v_exp_f32 v113, v113
QK 232,196,76
s_waitcnt lgkmcnt(7)
s_add_u32 m0, s60, 63232
RT 120,241,24576
CV 219,110,111
QK 128,104,80
RT 148,240,25616
v_mul_f32 v114, s71, v114
QK 128,108,84
buffer_load_dwordx4 v237, s[48:51], s77 offen offset:256 nt lds
s_nop 3
QK 144,128,80
s_waitcnt vmcnt(9)
RT 146,240,31744
RT 168,241,25600
QK 144,132,84
s_add_u32 m0, s60, 12288
RQ 92,245,6144
RT 132,241,26640
QK 160,152,80
s_add_u32 s56, s62, 55296
buffer_load_dwordx4 v237, s[44:47], s56 offen offset:0 nt lds
QK 160,156,84
RQ 88,244,6144
RT 174,241,31760
QK 176,176,80
s_add_u32 m0, s60, 13184
RT 154,240,43008
CV 218,108,109
QK 176,180,84
buffer_load_dwordx4 v237, s[44:47], s56 offen offset:128 nt lds
s_nop 3
PW 228,44,80
RT 124,241,24592
v_exp_f32 v112, v112
PW 228,60,84
s_add_u32 m0, s60, 14080
RT 172,241,25616
RT 178,241,43008
QK 232,200,80
buffer_load_dwordx4 v237, s[44:47], s56 offen offset:256 nt lds
QK 232,204,84
s_waitcnt lgkmcnt(5)
s_add_u32 s56, s62, 67584
RT 108,240,26640
RQ 196,239,43008
QK 128,112,88
RT 104,240,26624
v_exp_f32 v114, v114
QK 128,116,92
s_add_u32 m0, s60, 24576
RT 122,241,30720
RT 110,240,32784
s_nop 3
QK 144,136,88
RT 130,241,32768
CV 222,112,113
QK 144,140,92
buffer_load_dwordx4 v237, s[44:47], s56 offen offset:0 nt lds
QK 160,160,88
RT 176,241,36864
CV 223,114,115
QK 160,164,92
RT 106,240,32768
v_mul_f32 v128, s71, v128
v_mul_f32 v130, s71, v130
v_mul_f32 v131, s71, v131
QK 176,184,88
s_add_u32 m0, s60, 25472
RT 156,240,36880
v_exp_f32 v128, v128
v_exp_f32 v130, v130
QK 176,188,92
RT 134,241,32784
v_mul_f32 v146, s71, v146
v_mul_f32 v147, s71, v147
s_nop 3
PW 228,76,88
buffer_load_dwordx4 v237, s[44:47], s56 offen offset:128 nt lds
v_mul_f32 v129, s71, v129
v_exp_f32 v129, v129
v_exp_f32 v131, v131
v_mul_f32 v145, s71, v145
PW 228,92,92
RT 112,240,37888
CV 193,130,131
v_mul_f32 v144, s71, v144
v_exp_f32 v144, v144
v_exp_f32 v145, v145
v_exp_f32 v146, v146
v_exp_f32 v147, v147
QK 232,208,88
s_add_u32 m0, s60, 26368
RT 180,241,36880
CV 192,128,129
CV 194,144,145
CV 195,146,147
v_mul_f32 v162, s71, v162
QK 232,212,92
RT 128,241,26624
v_mul_f32 v160, s71, v160
v_mul_f32 v178, s71, v178
v_mul_f32 v179, s71, v179
v_exp_f32 v178, v178
PX 12,192,96
RT 116,240,37904
v_mul_f32 v161, s71, v161
v_mul_f32 v176, s71, v176
PX 28,192,100
s_waitcnt lgkmcnt(10)
)"
R"(
RT 152,240,36864
v_exp_f32 v161, v161
s_nop 3
PX 44,192,120
RT 114,240,44032
v_exp_f32 v179, v179
PX 60,192,124
buffer_load_dwordx4 v237, s[44:47], s56 offen offset:256 nt lds
v_mul_f32 v177, s71, v177
PX 76,192,144
RT 158,240,43024
v_exp_f32 v176, v176
v_exp_f32 v177, v177
PX 92,192,148
RQ 204,239,44032
v_exp_f32 v160, v160
v_exp_f32 v162, v162
PX 108,192,168
RT 142,241,44048
v_mul_f32 v163, s71, v163
PX 112,192,172
s_waitcnt lgkmcnt(11)
RT 138,241,44032
CV 210,176,177
PX 128,192,104
s_add_u32 m0, s61, 8192
RT 136,241,37888
v_exp_f32 v163, v163
PX 144,192,108
s_waitcnt lgkmcnt(8)
RT 182,241,43024
CV 208,160,161
CV 209,162,163
PX 160,192,128
s_add_u32 s58, s69, 0
RT 118,240,44048
CV 211,178,179
PX 176,192,132
buffer_load_dwordx4 v237, s[40:43], s58 offen offset:0 nt lds
v_mfma_f32_16x16x32_bf16 a[232:235], v[248:251], v[192:195], a[232:235]
s_waitcnt lgkmcnt(8)
RQ 208,238,45056
v_mul_f32 v229, s71, v229
PW 12,208,152
s_waitcnt lgkmcnt(7)
s_add_u32 m0, s61, 6144
RT 140,241,37904
v_mul_f32 v228, s71, v228
PW 28,208,156
buffer_load_dwordx4 v237, s[40:43], s58 offen offset:3072 nt lds
PW 44,208,176
s_waitcnt lgkmcnt(3)
s_add_u32 s58, s69, 0
RQ 200,238,44032
v_mul_f32 v230, s71, v230
PW 60,208,180
s_add_u32 m0, s61, 10112
RQ 192,238,43008
v_exp_f32 v230, v230
PW 76,208,112
s_waitcnt lgkmcnt(4)
buffer_load_dwordx4 v237, s[40:43], s58 offen offset:128 nt lds
PW 92,208,116
RQ 212,239,45056
v_mul_f32 v231, s71, v231
PW 108,208,136
s_waitcnt lgkmcnt(3)
v_exp_f32 v228, v228
PW 112,208,140
s_add_u32 m0, s61, 8064
v_exp_f32 v229, v229
PW 128,208,216
v_exp_f32 v231, v231
PW 144,208,220
s_waitcnt vmcnt(12) lgkmcnt(0)
buffer_load_dwordx4 v237, s[40:43], s58 offen offset:3200 nt lds
v_smfmac_f32_16x16x64_bf16 a[232:235], a[240:243], v[204:211], v236
s_barrier
CV 224,228,229
CV 225,230,231
PW 156,204,224
L_s3_%=:
v_mul_f32 v233, s71, v233
v_mul_f32 v234, s71, v234
PW 160,208,224
s_add_u32 m0, s60, 74624
RT 218,241,7168
v_mul_f32 v232, s71, v232
PW 176,208,228
s_add_u32 s57, s62, 49152
v_exp_f32 v234, v234
PW 172,204,228
RT 224,240,2048
v_mul_f32 v235, s71, v235
PW 168,200,228
buffer_load_dwordx4 v237, s[48:51], s57 offen offset:128 nt lds
PW 164,196,228
v_exp_f32 v233, v233
QC 228,212,20
RT 222,241,7184
QK 228,208,16
s_add_u32 m0, s60, 75520
v_exp_f32 v235, v235
QK 228,204,12
v_exp_f32 v232, v232
QK 228,200,8
s_add_u32 s58, s69, 0
L_c2_%=:
s_cbranch_execz L_c6_%=
RT 216,241,1024
CV 227,234,235
QK 228,196,4
RT 220,241,1040
CV 226,232,233
QK 228,192,0
RT 226,240,8192
QC 232,192,24
buffer_load_dwordx4 v237, s[48:51], s57 offen offset:256 nt lds
QK 232,196,28
RT 228,240,2064
QK 232,200,32
RT 230,240,8208
QK 232,204,36
v_mul_f32 v230, s71, v230
v_mul_f32 v231, s71, v231
QK 232,208,40
v_mul_f32 v228, s71, v228
v_mul_f32 v229, s71, v229
QK 232,212,44
s_add_u32 s56, s62, 79872
v_exp_f32 v228, v228
v_smfmac_f32_16x16x64_bf16 a[232:235], a[236:239], v[212:219], v236
s_waitcnt lgkmcnt(2)
v_exp_f32 v229, v229
PW 96,212,216
v_exp_f32 v231, v231
CV 180,228,229
PW 100,216,216
s_add_u32 m0, s60, 73728
v_exp_f32 v230, v230
v_mfma_f32_16x16x32_bf16 a[244:247], v[212:215], a[220:223], a[244:247]
buffer_load_dwordx4 v237, s[48:51], s57 offen offset:0 nt lds
v_mfma_f32_16x16x32_bf16 a[248:251], v[216:219], a[220:223], a[248:251]
CV 181,230,231
v_mul_f32 v234, s71, v234
QC 228,212,68
s_add_u32 m0, s60, 86016
v_mul_f32 v232, s71, v232
v_mul_f32 v235, s71, v235
QK 228,208,64
v_mul_f32 v233, s71, v233
v_exp_f32 v232, v232
QK 228,204,60
s_add_u32 s77, s62, 61440
v_exp_f32 v233, v233
QK 228,200,56
v_exp_f32 v235, v235
QK 228,196,52
v_exp_f32 v234, v234
QK 228,192,48
CV 184,232,233
CV 185,234,235
QC 232,192,72
buffer_load_dwordx4 v237, s[48:51], s77 offen offset:0 nt lds
QK 232,196,76
RQ 196,239,0
QK 232,200,80
RQ 200,238,1024
QK 232,204,84
RQ 204,239,1024
v_mul_f32 v229, s71, v229
QK 232,208,88
RQ 208,238,2048
v_mul_f32 v228, s71, v228
v_mul_f32 v230, s71, v230
QK 232,212,92
RQ 212,239,2048
v_mul_f32 v231, s71, v231
PW 104,220,216
s_add_u32 m0, s61, 12032
RQ 192,238,0
v_exp_f32 v231, v231
PW 108,224,216
v_exp_f32 v229, v229
v_mfma_f32_16x16x32_bf16 a[252:255], v[220:223], a[220:223], a[252:255]
v_exp_f32 v228, v228
PW 112,224,220
buffer_load_dwordx4 v237, s[40:43], s58 offen offset:256 nt lds
PW 128,224,224
v_exp_f32 v230, v230
v_mul_f32 v234, s71, v234
PW 124,220,224
s_waitcnt lgkmcnt(0)
CV 188,228,229
CV 189,230,231
QC 228,212,20
v_mul_f32 v233, s71, v233
v_mul_f32 v235, s71, v235
QK 228,208,16
v_mul_f32 v232, s71, v232
v_exp_f32 v232, v232
QK 228,204,12
v_exp_f32 v234, v234
QK 228,200,8
v_exp_f32 v235, v235
QK 228,196,4
s_add_u32 m0, s60, 36864
v_exp_f32 v233, v233
QK 228,192,0
CV 192,232,233
CV 193,234,235
QC 232,192,24
buffer_load_dwordx4 v237, s[44:47], s56 offen offset:0 nt lds
QK 232,196,28
RT 218,241,8192
QK 232,200,32
RT 216,241,2048
QK 232,204,36
s_add_u32 m0, s60, 37760
v_mul_f32 v230, s71, v230
v_mul_f32 v231, s71, v231
QK 232,208,40
v_mul_f32 v228, s71, v228
v_mul_f32 v229, s71, v229
QK 232,212,44
v_exp_f32 v228, v228
v_smfmac_f32_16x16x64_bf16 a[232:235], a[240:243], v[220:227], v236
v_exp_f32 v231, v231
PW 144,224,228
v_exp_f32 v229, v229
PW 140,220,228
buffer_load_dwordx4 v237, s[44:47], s56 offen offset:128 nt lds
PW 120,216,224
v_exp_f32 v230, v230
CV 182,228,229
PW 116,212,224
CV 183,230,231
v_mul_f32 v232, s71, v232
v_mul_f32 v233, s71, v233
QC 228,212,68
v_exp_f32 v232, v232
QK 228,208,64
v_mul_f32 v235, s71, v235
v_exp_f32 v233, v233
QK 228,204,60
s_add_u32 m0, s61, 9984
RT 222,241,8208
v_mul_f32 v234, s71, v234
QK 228,200,56
v_exp_f32 v234, v234
QK 228,196,52
v_exp_f32 v235, v235
QK 228,192,48
CV 186,232,233
CV 187,234,235
QC 232,192,72
buffer_load_dwordx4 v237, s[40:43], s58 offen offset:3328 nt lds
QK 232,196,76
RT 220,241,2064
QK 232,200,80
RQ 196,239,6144
QK 232,204,84
RQ 204,239,7168
v_mul_f32 v228, s71, v228
QK 232,208,88
RQ 200,238,7168
QK 232,212,92
s_waitcnt lgkmcnt(4)
RQ 208,238,8192
v_mul_f32 v229, s71, v229
PW 136,216,228
s_add_u32 s57, s62, 73728
RQ 192,238,6144
v_mul_f32 v230, s71, v230
PW 132,212,228
s_add_u32 m0, s60, 38656
RQ 212,239,8192
v_mul_f32 v231, s71, v231
PW 148,212,216
v_exp_f32 v230, v230
PW 152,216,216
s_waitcnt lgkmcnt(4)
buffer_load_dwordx4 v237, s[44:47], s56 offen offset:256 nt lds
PW 164,212,220
v_exp_f32 v231, v231
PW 168,216,220
v_exp_f32 v229, v229
PW 156,220,216
s_waitcnt vmcnt(16) lgkmcnt(0)
v_exp_f32 v228, v228
PW 160,224,216
s_barrier
PW 172,220,220
L_s4_%=:
CV 191,230,231
v_mul_f32 v234, s71, v234
PW 176,224,220
CV 190,228,229
v_mul_f32 v232, s71, v232
QC 228,212,20
v_mul_f32 v233, s71, v233
v_mul_f32 v235, s71, v235
QK 228,208,16
v_exp_f32 v232, v232
QK 228,204,12
s_add_u32 s58, s69, 6144
v_exp_f32 v233, v233
QK 228,200,8
v_exp_f32 v235, v235
QK 228,196,4
v_exp_f32 v234, v234
QK 228,192,0
CV 194,232,233
CV 195,234,235
QC 232,192,24
RT 228,240,12288
QK 232,196,28
RT 226,240,6144
QK 232,200,32
QK 232,204,36
RT 224,240,0
v_mul_f32 v228, s71, v228
QK 232,208,40
v_mul_f32 v229, s71, v229
v_mul_f32 v230, s71, v230
QK 232,212,44
RT 218,240,6160
v_mul_f32 v231, s71, v231
QC 248,212,68
v_exp_f32 v231, v231
QK 248,208,64
s_add_u32 m0, s60, 86912
v_exp_f32 v228, v228
QK 248,204,60
v_exp_f32 v229, v229
QK 248,200,56
v_exp_f32 v230, v230
QK 248,196,52
CV 196,228,229
CV 197,230,231
QK 248,192,48
buffer_load_dwordx4 v237, s[48:51], s77 offen offset:128 nt lds
QC 228,192,72
RT 230,240,18432
QK 228,196,76
RQ 196,239,12288
v_mul_f32 v235, s71, v235
QK 228,200,80
RQ 192,238,12288
v_mul_f32 v234, s71, v234
QK 228,204,84
RQ 204,239,13312
QK 228,208,88
RT 220,240,12304
v_mul_f32 v232, s71, v232
QK 228,212,92
RT 216,240,16
v_mul_f32 v249, s71, v249
v_smfmac_f32_16x16x64_bf16 a[232:235], a[236:239], v[180:187], v236
s_waitcnt lgkmcnt(5)
RT 222,240,18448
v_mul_f32 v248, s71, v248
PW 0,212,224
s_add_u32 m0, s60, 87808
v_exp_f32 v235, v235
PW 0,180,228
buffer_load_dwordx4 v237, s[48:51], s77 offen offset:256 nt lds
PW 4,216,224
RQ 200,238,13312
PW 4,184,228
v_mul_f32 v250, s71, v250
PW 8,220,224
RQ 208,238,14336
PW 8,188,228
v_exp_f32 v249, v249
PW 12,224,224
RT 224,241,0
v_mul_f32 v231, s71, v231
PW 12,192,228
s_waitcnt lgkmcnt(3)
RT 226,241,6144
PW 28,224,216
s_add_u32 m0, s60, 50944
RT 230,241,18432
v_mul_f32 v230, s71, v230
PW 28,192,220
RT 228,241,12288
PW 24,220,216
v_exp_f32 v231, v231
PW 24,188,220
buffer_load_dwordx4 v237, s[48:51], s57 offen offset:256 nt lds
PW 20,216,216
RQ 212,239,14336
v_mul_f32 v233, s71, v233
PW 20,184,220
v_exp_f32 v232, v232
PW 16,212,216
v_exp_f32 v233, v233
PW 16,180,220
s_waitcnt lgkmcnt(1)
)"
R"(
v_exp_f32 v248, v248
PW 32,212,224
v_exp_f32 v234, v234
PW 32,180,228
v_exp_f32 v230, v230
PW 36,216,224
s_add_u32 m0, s61, 14336
RT 222,241,18448
v_mul_f32 v228, s71, v228
PW 36,184,228
CV 201,234,235
CV 209,230,231
PW 40,220,224
buffer_load_dwordx4 v237, s[40:43], s58 offen offset:0 nt lds
PW 40,188,228
RT 220,241,12304
CV 204,248,249
PW 44,224,224
v_exp_f32 v250, v250
PW 44,192,228
s_waitcnt lgkmcnt(2)
RT 216,241,16
CV 200,232,233
QC 232,212,20
RT 224,240,1024
v_mul_f32 v251, s71, v251
QK 232,208,16
v_exp_f32 v228, v228
QK 232,204,12
s_add_u32 m0, s60, 0
RT 218,241,6160
QK 232,200,8
s_add_u32 s56, s62, 92160
v_exp_f32 v251, v251
QK 232,196,4
CV 205,250,251
QK 232,192,0
v_mul_f32 v229, s71, v229
QC 248,192,24
s_waitcnt vmcnt(15)
buffer_load_dwordx4 v237, s[44:47], s56 offen offset:0 nt lds
QK 248,196,28
v_exp_f32 v229, v229
QK 248,200,32
RT 226,240,7168
QK 248,204,36
v_mul_f32 v232, s71, v232
v_mul_f32 v233, s71, v233
QK 248,208,40
CV 208,228,229
v_mul_f32 v235, s71, v235
QK 248,212,44
v_exp_f32 v235, v235
QC 228,212,68
s_add_u32 m0, s60, 896
v_mul_f32 v234, s71, v234
QK 228,208,64
v_exp_f32 v233, v233
QK 228,204,60
v_exp_f32 v234, v234
QK 228,200,56
buffer_load_dwordx4 v237, s[44:47], s56 offen offset:128 nt lds
QK 228,196,52
v_exp_f32 v232, v232
QK 228,192,48
CV 198,232,233
CV 199,234,235
QC 232,192,72
RT 230,240,19456
QK 232,196,76
RT 228,240,13312
v_mul_f32 v251, s71, v251
QK 232,200,80
RQ 96,244,8192
v_mul_f32 v248, s71, v248
QK 232,204,84
RQ 196,239,18432
v_mul_f32 v249, s71, v249
QK 232,208,88
s_add_u32 m0, s61, 12288
RQ 204,239,19456
v_mul_f32 v231, s71, v231
QK 232,212,92
RQ 208,238,20480
v_mul_f32 v230, s71, v230
v_smfmac_f32_16x16x64_bf16 a[232:235], a[240:243], v[188:195], v236
s_waitcnt lgkmcnt(5)
v_exp_f32 v231, v231
PW 60,224,216
buffer_load_dwordx4 v237, s[40:43], s58 offen offset:3072 nt lds
PW 60,192,220
PW 56,220,216
RQ 100,245,8192
v_mul_f32 v235, s71, v235
PW 56,188,220
RQ 192,238,18432
v_mul_f32 v232, s71, v232
PW 52,216,216
v_mul_f32 v250, s71, v250
PW 52,184,220
s_add_u32 m0, s60, 1792
v_exp_f32 v250, v250
PW 48,212,216
RT 216,240,1040
v_mul_f32 v234, s71, v234
PW 48,180,220
RT 220,240,13328
v_mul_f32 v233, s71, v233
PW 64,212,224
s_waitcnt lgkmcnt(3)
s_add_u32 s58, s69, 6144
RT 218,240,7184
PW 64,180,228
RT 222,240,19472
v_mul_f32 v229, s71, v229
PW 68,216,224
buffer_load_dwordx4 v237, s[44:47], s56 offen offset:256 nt lds
PW 68,184,228
RQ 212,239,20480
PW 72,220,224
RQ 200,238,19456
v_mul_f32 v228, s71, v228
PW 72,188,228
v_exp_f32 v230, v230
PW 76,224,224
s_waitcnt lgkmcnt(3)
v_exp_f32 v251, v251
PW 76,192,228
s_add_u32 s77, s62, 86016
v_exp_f32 v228, v228
PW 92,224,216
s_waitcnt lgkmcnt(2)
v_exp_f32 v229, v229
PW 92,192,220
s_add_u32 m0, s60, 50048
CV 207,230,231
PW 88,220,216
CV 206,228,229
PW 88,188,220
v_exp_f32 v248, v248
PW 84,216,216
s_waitcnt vmcnt(15) lgkmcnt(0)
v_exp_f32 v233, v233
PW 84,184,220
s_barrier
PW 80,212,216
L_s5_%=:
RT 228,241,25600
PW 80,180,220
v_exp_f32 v249, v249
QC 228,212,20
v_exp_f32 v232, v232
QK 228,208,16
CV 203,250,251
CV 210,232,233
QK 228,204,12
v_exp_f32 v234, v234
QK 228,200,8
CV 202,248,249
QK 228,196,4
v_exp_f32 v235, v235
QK 228,192,0
RT 216,241,13328
CV 211,234,235
QC 232,192,24
s_waitcnt vmcnt(17)
RT 230,241,31744
QK 232,196,28
RT 222,241,31760
QK 232,200,32
RT 224,241,13312
QK 232,204,36
v_mul_f32 v228, s71, v228
v_mul_f32 v231, s71, v231
QK 232,208,40
RT 226,241,19456
v_mul_f32 v230, s71, v230
QK 232,212,44
v_exp_f32 v228, v228
QC 248,212,68
v_exp_f32 v230, v230
QK 248,208,64
v_exp_f32 v231, v231
QK 248,204,60
v_mul_f32 v229, s71, v229
CV 213,230,231
QK 248,200,56
v_exp_f32 v229, v229
QK 248,196,52
RT 220,241,25616
CV 212,228,229
QK 248,192,48
RQ 108,245,10240
v_mul_f32 v232, s71, v232
QC 228,192,72
buffer_load_dwordx4 v237, s[48:51], s57 offen offset:128 nt lds
QK 228,196,76
s_add_u32 m0, s60, 49152
RQ 104,244,10240
QK 228,200,80
v_exp_f32 v232, v232
QK 228,204,84
v_mul_f32 v250, s71, v250
QK 228,208,88
v_mul_f32 v235, s71, v235
QK 228,212,92
RT 218,241,19472
v_mul_f32 v234, s71, v234
v_smfmac_f32_16x16x64_bf16 a[232:235], a[236:239], v[196:203], v236
s_waitcnt lgkmcnt(2)
RQ 196,239,24576
PW 96,180,224
RQ 212,239,26624
PW 96,196,228
RQ 204,239,25600
v_mul_f32 v233, s71, v233
PW 100,184,224
buffer_load_dwordx4 v237, s[48:51], s57 offen offset:0 nt lds
PW 100,200,228
v_exp_f32 v250, v250
PW 104,188,224
v_mul_f32 v229, s71, v229
PW 104,204,228
PW 108,192,224
RT 226,240,20480
PW 108,208,228
s_waitcnt lgkmcnt(1)
RT 224,240,14336
PW 112,192,216
RT 230,240,32768
v_mul_f32 v251, s71, v251
PW 112,208,220
RT 228,240,26624
v_mfma_f32_16x16x32_bf16 a[252:255], v[188:191], a[216:219], a[252:255]
s_add_u32 m0, s60, 61440
v_exp_f32 v234, v234
v_mfma_f32_16x16x32_bf16 a[252:255], v[204:207], a[220:223], a[252:255]
v_mul_f32 v249, s71, v249
v_mul_f32 v228, s71, v228
v_mfma_f32_16x16x32_bf16 a[248:251], v[184:187], a[216:219], a[248:251]
buffer_load_dwordx4 v237, s[48:51], s77 offen offset:0 nt lds
v_mfma_f32_16x16x32_bf16 a[248:251], v[200:203], a[220:223], a[248:251]
v_exp_f32 v233, v233
v_mfma_f32_16x16x32_bf16 a[244:247], v[180:183], a[216:219], a[244:247]
RQ 208,238,26624
v_mul_f32 v248, s71, v248
v_mfma_f32_16x16x32_bf16 a[244:247], v[196:199], a[220:223], a[244:247]
s_waitcnt lgkmcnt(1)
RT 222,240,32784
v_mul_f32 v230, s71, v230
PW 116,180,224
s_add_u32 s56, s62, 104448
v_exp_f32 v229, v229
PW 116,196,228
s_add_u32 m0, s61, 3968
RQ 200,238,25600
CV 216,232,233
PW 120,184,224
v_exp_f32 v235, v235
PW 120,200,228
s_add_u32 s57, s62, 98304
RQ 192,238,24576
v_mul_f32 v231, s71, v231
PW 124,188,224
v_exp_f32 v249, v249
PW 124,204,228
v_exp_f32 v248, v248
PW 128,192,224
buffer_load_dwordx4 v237, s[40:43], s58 offen offset:128 nt lds
PW 128,208,228
s_add_u32 m0, s60, 12288
RT 228,241,26624
CV 217,234,235
QC 232,212,20
s_waitcnt lgkmcnt(2)
v_exp_f32 v251, v251
QK 232,208,16
RT 216,240,14352
CV 220,248,249
QK 232,204,12
v_exp_f32 v228, v228
QK 232,200,8
v_exp_f32 v230, v230
QK 232,196,4
s_waitcnt lgkmcnt(2)
CV 224,228,229
QK 232,192,0
RT 226,241,20480
CV 221,250,251
QC 248,192,24
s_waitcnt vmcnt(13)
v_exp_f32 v231, v231
QK 248,196,28
RT 218,240,20496
CV 225,230,231
QK 248,200,32
buffer_load_dwordx4 v237, s[44:47], s56 offen offset:0 lds
QK 248,204,36
v_mul_f32 v233, s71, v233
v_mul_f32 v235, s71, v235
QK 248,208,40
v_mul_f32 v232, s71, v232
v_mul_f32 v234, s71, v234
QK 248,212,44
v_exp_f32 v235, v235
QC 228,212,68
v_exp_f32 v232, v232
QK 228,208,64
v_exp_f32 v233, v233
QK 228,204,60
RT 224,241,14336
CV 214,232,233
QK 228,200,56
RQ 116,245,12288
QK 228,196,52
s_add_u32 m0, s60, 13184
v_exp_f32 v234, v234
QK 228,192,48
RT 220,240,26640
L_c3_%=:
s_cbranch_execz L_c7_%=
CV 215,234,235
QC 232,192,72
buffer_load_dwordx4 v237, s[44:47], s56 offen offset:128 lds
QK 232,196,76
RQ 192,238,30720
QK 232,200,80
s_add_u32 m0, s61, 1920
RQ 200,238,31744
v_mul_f32 v248, s71, v248
QK 232,204,84
RQ 196,239,30720
QK 232,208,88
RT 230,241,32768
v_mul_f32 v230, s71, v230
QK 232,212,92
v_mul_f32 v251, s71, v251
v_mul_f32 v231, s71, v231
v_smfmac_f32_16x16x64_bf16 a[232:235], a[240:243], v[204:211], v236
s_waitcnt lgkmcnt(4)
RQ 112,244,12288
v_mul_f32 v250, s71, v250
PW 144,192,216
v_exp_f32 v230, v230
PW 144,208,220
v_exp_f32 v231, v231
PW 140,188,216
buffer_load_dwordx4 v237, s[40:43], s58 offen offset:3200 nt lds
PW 140,204,220
v_mul_f32 v249, s71, v249
v_mul_f32 v229, s71, v229
PW 136,184,216
RQ 212,239,32768
PW 136,200,220
RQ 204,239,31744
PW 132,180,216
s_add_u32 m0, s60, 14080
RT 218,241,20496
v_mul_f32 v228, s71, v228
PW 132,196,220
RT 216,241,14352
v_mul_f32 v234, s71, v234
PW 148,180,224
s_waitcnt lgkmcnt(4)
RT 220,241,26640
v_mul_f32 v235, s71, v235
PW 148,196,228
RT 222,241,32784
CV 223,230,231
PW 152,184,224
RQ 208,238,32768
PW 152,200,228
v_mul_f32 v233, s71, v233
PW 156,188,224
buffer_load_dwordx4 v237, s[44:47], s56 offen offset:256 lds
PW 156,204,228
v_exp_f32 v235, v235
PW 160,192,224
v_exp_f32 v233, v233
PW 160,208,228
s_waitcnt lgkmcnt(1)
v_exp_f32 v250, v250
PW 176,192,216
v_exp_f32 v234, v234
PW 176,208,220
s_add_u32 m0, s60, 62336
v_exp_f32 v229, v229
PW 172,188,216
v_exp_f32 v251, v251
PW 172,204,220
v_exp_f32 v228, v228
PW 168,184,216
s_waitcnt vmcnt(15) lgkmcnt(0)
v_exp_f32 v249, v249
PW 168,200,220
s_barrier
PW 164,180,216
L_s6_%=:
buffer_load_dwordx4 v237, s[48:51], s77 offen offset:128 nt lds
PW 164,196,220
)"
R"(
CV 222,228,229
QC 228,212,20
s_add_u32 m0, s60, 63232
v_mul_f32 v232, s71, v232
QK 228,208,16
RT 218,240,30736
CV 219,250,251
QK 228,204,12
RT 230,240,43008
QK 228,200,8
s_add_u32 s58, s69, 6144
v_exp_f32 v232, v232
QK 228,196,4
CV 227,234,235
QK 228,192,0
RT 222,240,43024
CV 226,232,233
QC 232,192,24
v_exp_f32 v248, v248
QK 232,196,28
RT 216,240,24592
CV 218,248,249
QK 232,200,32
buffer_load_dwordx4 v237, s[48:51], s77 offen offset:256 nt lds
QK 232,204,36
v_mul_f32 v229, s71, v229
v_mul_f32 v230, s71, v230
QK 232,208,40
s_add_u32 m0, s60, 75520
v_exp_f32 v229, v229
QK 232,212,44
RT 228,240,36864
v_mul_f32 v231, s71, v231
QC 248,212,68
RT 224,240,24576
QK 248,208,64
v_exp_f32 v230, v230
QK 248,204,60
v_exp_f32 v231, v231
QK 248,200,56
v_mul_f32 v228, s71, v228
CV 181,230,231
QK 248,196,52
v_exp_f32 v228, v228
QK 248,192,48
RT 226,240,30720
CV 180,228,229
QC 228,192,72
buffer_load_dwordx4 v237, s[48:51], s57 offen offset:256 lds
QK 228,196,76
v_mul_f32 v234, s71, v234
v_mul_f32 v235, s71, v235
QK 228,200,80
v_exp_f32 v235, v235
QK 228,204,84
RQ 204,239,37888
QK 228,208,88
s_add_u32 m0, s61, 5888
RT 220,240,36880
QK 228,212,92
v_smfmac_f32_16x16x64_bf16 a[232:235], a[236:239], v[212:219], v236
s_waitcnt lgkmcnt(2)
v_mul_f32 v232, s71, v232
v_mul_f32 v233, s71, v233
PW 0,196,224
PW 0,212,228
RQ 192,238,36864
v_mul_f32 v251, s71, v251
PW 4,200,224
buffer_load_dwordx4 v237, s[40:43], s58 offen offset:256 nt lds
PW 4,216,228
s_add_u32 m0, s60, 24576
RQ 196,239,36864
v_mul_f32 v249, s71, v249
PW 8,204,224
s_add_u32 s56, s62, 116736
v_mul_f32 v231, s71, v231
PW 8,220,228
v_exp_f32 v232, v232
PW 12,208,224
RT 226,241,30720
PW 12,224,228
RT 224,241,24576
v_mul_f32 v229, s71, v229
PW 28,208,216
s_waitcnt lgkmcnt(3)
RT 230,241,43008
v_mul_f32 v230, s71, v230
PW 28,224,220
RT 228,241,36864
PW 24,204,216
v_exp_f32 v234, v234
PW 24,220,220
v_mul_f32 v250, s71, v250
v_mul_f32 v228, s71, v228
PW 20,200,216
buffer_load_dwordx4 v237, s[44:47], s56 offen offset:0 lds
PW 20,216,220
s_add_u32 m0, s60, 25472
RQ 212,239,38912
PW 16,196,216
RQ 208,238,38912
CV 185,234,235
PW 16,212,220
s_waitcnt lgkmcnt(2)
v_exp_f32 v249, v249
PW 32,196,224
v_exp_f32 v231, v231
PW 32,212,228
v_exp_f32 v251, v251
PW 36,200,224
v_exp_f32 v250, v250
PW 36,216,228
RQ 200,238,37888
PW 40,204,224
v_exp_f32 v229, v229
PW 40,220,228
buffer_load_dwordx4 v237, s[44:47], s56 offen offset:128 lds
PW 44,208,224
v_exp_f32 v233, v233
PW 44,224,228
s_waitcnt lgkmcnt(1)
CV 184,232,233
v_mul_f32 v248, s71, v248
QC 232,212,20
v_exp_f32 v230, v230
QK 232,208,16
v_exp_f32 v228, v228
QK 232,204,12
s_waitcnt lgkmcnt(0)
v_exp_f32 v248, v248
QK 232,200,8
RT 218,241,30736
CV 188,248,249
QK 232,196,4
CV 189,250,251
QK 232,192,0
s_add_u32 s77, s62, 110592
RT 216,241,24592
CV 192,228,229
QC 248,192,24
s_waitcnt vmcnt(15)
CV 193,230,231
QK 248,196,28
RT 228,240,37888
QK 248,200,32
RT 222,241,43024
QK 248,204,36
v_mul_f32 v233, s71, v233
v_mul_f32 v234, s71, v234
QK 248,208,40
RT 224,240,25600
v_mul_f32 v232, s71, v232
QK 248,212,44
v_exp_f32 v232, v232
QC 228,212,68
RT 230,240,44032
v_mul_f32 v235, s71, v235
QK 228,208,64
s_add_u32 m0, s61, 3840
v_exp_f32 v234, v234
QK 228,204,60
v_exp_f32 v235, v235
QK 228,200,56
RT 220,241,36880
v_mul_f32 v251, s71, v251
QK 228,196,52
v_exp_f32 v233, v233
QK 228,192,48
CV 182,232,233
CV 183,234,235
QC 232,192,72
RT 226,240,31744
QK 232,196,76
RQ 120,244,14336
QK 232,200,80
v_mul_f32 v248, s71, v248
QK 232,204,84
RQ 124,245,14336
QK 232,208,88
RQ 196,239,43008
v_mul_f32 v229, s71, v229
QK 232,212,92
RQ 212,239,45056
v_mul_f32 v230, s71, v230
v_smfmac_f32_16x16x64_bf16 a[232:235], a[240:243], v[220:227], v236
s_waitcnt lgkmcnt(4)
RQ 204,239,44032
v_mul_f32 v228, s71, v228
PW 60,208,216
RQ 200,238,44032
PW 60,224,220
v_exp_f32 v248, v248
PW 56,204,216
v_mul_f32 v231, s71, v231
PW 56,220,220
buffer_load_dwordx4 v237, s[40:43], s58 offen offset:3328 nt lds
PW 52,200,216
RQ 208,238,45056
PW 52,216,220
s_add_u32 m0, s60, 26368
RQ 192,238,43008
v_mul_f32 v250, s71, v250
PW 48,196,216
RT 218,240,31760
PW 48,212,220
RT 222,240,44048
v_mul_f32 v235, s71, v235
PW 64,196,224
RT 216,240,25616
v_mul_f32 v249, s71, v249
PW 64,212,228
RT 220,240,37904
v_mul_f32 v233, s71, v233
PW 68,200,224
v_mul_f32 v232, s71, v232
PW 68,216,228
s_add_u32 s58, s69, 12288
v_exp_f32 v231, v231
PW 72,204,224
v_exp_f32 v228, v228
PW 72,220,228
buffer_load_dwordx4 v237, s[44:47], s56 offen offset:256 lds
PW 76,208,224
s_add_u32 m0, s60, 74624
v_exp_f32 v230, v230
PW 76,224,228
s_waitcnt lgkmcnt(0)
v_exp_f32 v229, v229
PW 92,208,216
v_mul_f32 v234, s71, v234
PW 92,224,220
v_exp_f32 v233, v233
PW 88,204,216
v_exp_f32 v235, v235
PW 88,220,220
CV 190,228,229
CV 191,230,231
PW 84,200,216
s_waitcnt vmcnt(15)
v_exp_f32 v250, v250
PW 84,216,220
s_barrier
PW 80,196,216
L_s7_%=:
RT 226,241,44032
PW 80,212,220
v_exp_f32 v251, v251
QC 228,212,20
v_exp_f32 v234, v234
QK 228,208,16
v_exp_f32 v249, v249
QK 228,204,12
v_exp_f32 v232, v232
QK 228,200,8
RT 216,241,37904
QK 228,196,4
CV 187,250,251
CV 194,232,233
QK 228,192,0
RT 222,241,7184
CV 195,234,235
QC 232,192,24
CV 186,248,249
QK 232,196,28
RT 224,241,37888
QK 232,200,32
buffer_load_dwordx4 v237, s[48:51], s57 offen offset:128 lds
QK 232,204,36
v_mul_f32 v230, s71, v230
v_mul_f32 v231, s71, v231
QK 232,208,40
s_add_u32 m0, s60, 73728
RT 228,241,1024
QK 232,212,44
v_exp_f32 v230, v230
QC 248,212,68
v_mul_f32 v228, s71, v228
v_mul_f32 v229, s71, v229
QK 248,208,64
v_exp_f32 v229, v229
QK 248,204,60
v_exp_f32 v228, v228
QK 248,200,56
v_exp_f32 v231, v231
QK 248,196,52
CV 196,228,229
CV 197,230,231
QK 248,192,48
buffer_load_dwordx4 v237, s[48:51], s57 offen offset:0 lds
QC 228,192,72
RT 230,241,7168
QK 228,196,76
RT 220,241,1040
v_mul_f32 v234, s71, v234
QK 228,200,80
RQ 196,239,0
v_mul_f32 v232, s71, v232
QK 228,204,84
v_exp_f32 v232, v232
QK 228,208,88
v_exp_f32 v234, v234
QK 228,212,92
v_smfmac_f32_16x16x64_bf16 a[232:235], a[236:239], v[180:187], v236
s_waitcnt lgkmcnt(2)
s_add_u32 m0, s60, 86016
RT 218,241,44048
PW 96,212,224
RQ 212,239,2048
v_mul_f32 v235, s71, v235
PW 96,180,228
buffer_load_dwordx4 v237, s[48:51], s77 offen offset:0 lds
PW 100,216,224
v_mul_f32 v233, s71, v233
PW 100,184,228
RQ 192,238,0
PW 104,220,224
v_exp_f32 v233, v233
PW 104,188,228
s_add_u32 s56, s62, 129024
v_mul_f32 v248, s71, v248
v_mul_f32 v250, s71, v250
PW 108,224,224
RT 224,240,38912
v_mul_f32 v249, s71, v249
PW 108,192,228
s_waitcnt lgkmcnt(2)
RT 230,240,8192
v_mul_f32 v229, s71, v229
PW 112,224,216
s_add_u32 m0, s61, 8192
RT 226,240,45056
PW 112,192,220
RT 228,240,2048
v_mfma_f32_16x16x32_bf16 a[252:255], v[220:223], a[216:219], a[252:255]
buffer_load_dwordx4 v237, s[40:43], s58 offen offset:0 lds
v_mfma_f32_16x16x32_bf16 a[252:255], v[188:191], a[220:223], a[252:255]
v_exp_f32 v235, v235
v_mfma_f32_16x16x32_bf16 a[248:251], v[216:219], a[216:219], a[248:251]
RQ 208,238,2048
v_mfma_f32_16x16x32_bf16 a[248:251], v[184:187], a[220:223], a[248:251]
s_add_u32 m0, s60, 36864
v_exp_f32 v249, v249
v_mfma_f32_16x16x32_bf16 a[244:247], v[212:215], a[216:219], a[244:247]
v_mul_f32 v251, s71, v251
v_mfma_f32_16x16x32_bf16 a[244:247], v[180:183], a[220:223], a[244:247]
s_waitcnt lgkmcnt(1)
RQ 204,239,1024
CV 200,232,233
PW 116,212,224
v_exp_f32 v248, v248
PW 116,180,228
v_exp_f32 v250, v250
PW 120,216,224
buffer_load_dwordx4 v237, s[44:47], s56 offen offset:0 lds
PW 120,184,228
RQ 200,238,1024
CV 204,248,249
PW 124,220,224
v_exp_f32 v251, v251
PW 124,188,228
v_mul_f32 v230, s71, v230
PW 128,224,224
CV 201,234,235
PW 128,192,228
v_exp_f32 v229, v229
QC 232,212,20
s_waitcnt lgkmcnt(1)
RT 224,241,38912
CV 205,250,251
QK 232,208,16
RT 220,240,2064
QK 232,204,12
s_waitcnt lgkmcnt(2)
RT 218,240,45072
v_mul_f32 v231, s71, v231
QK 232,200,8
v_exp_f32 v231, v231
QK 232,196,4
RT 226,241,45056
v_mul_f32 v228, s71, v228
QK 232,192,0
RT 230,241,8192
QC 248,192,24
s_waitcnt vmcnt(14)
RT 222,240,8208
QK 248,196,28
v_exp_f32 v228, v228
QK 248,200,32
v_exp_f32 v230, v230
QK 248,204,36
RT 216,240,38928
CV 209,230,231
QK 248,208,40
CV 208,228,229
v_mul_f32 v234, s71, v234
QK 248,212,44
RQ 128,244,4096
v_mul_f32 v232, s71, v232
QC 228,212,68
)"
R"(
v_mul_f32 v233, s71, v233
v_mul_f32 v235, s71, v235
QK 228,208,64
s_add_u32 m0, s60, 37760
v_exp_f32 v232, v232
QK 228,204,60
v_exp_f32 v235, v235
QK 228,200,56
v_exp_f32 v234, v234
QK 228,196,52
v_exp_f32 v233, v233
QK 228,192,48
CV 198,232,233
CV 199,234,235
QC 232,192,72
buffer_load_dwordx4 v237, s[44:47], s56 offen offset:128 lds
QK 232,196,76
RQ 196,239,6144
v_mul_f32 v249, s71, v249
QK 232,200,80
v_exp_f32 v249, v249
QK 232,204,84
RQ 192,238,6144
v_mul_f32 v229, s71, v229
QK 232,208,88
s_add_u32 s57, s62, 122880
RT 228,241,2048
v_mul_f32 v230, s71, v230
QK 232,212,92
RQ 212,239,8192
v_smfmac_f32_16x16x64_bf16 a[232:235], a[240:243], v[188:195], v236
s_waitcnt lgkmcnt(4)
v_mul_f32 v228, s71, v228
PW 144,224,216
PW 144,192,220
s_add_u32 m0, s61, 6144
RQ 208,238,8192
v_mul_f32 v248, s71, v248
PW 140,220,216
RQ 132,245,4096
PW 140,188,220
buffer_load_dwordx4 v237, s[40:43], s58 offen offset:3072 lds
PW 136,216,216
s_add_u32 m0, s60, 38656
RQ 200,238,7168
v_mul_f32 v232, s71, v232
PW 136,184,220
v_exp_f32 v230, v230
PW 132,212,216
RT 216,241,38928
v_mul_f32 v231, s71, v231
PW 132,180,220
RT 220,241,2064
v_mul_f32 v251, s71, v251
PW 148,212,224
s_waitcnt lgkmcnt(3)
RT 218,241,45072
PW 148,180,228
RT 222,241,8208
v_mul_f32 v250, s71, v250
PW 152,216,224
v_exp_f32 v231, v231
PW 152,184,228
RQ 204,239,7168
PW 156,220,224
v_mul_f32 v235, s71, v235
PW 156,188,228
buffer_load_dwordx4 v237, s[44:47], s56 offen offset:256 lds
PW 160,224,224
v_exp_f32 v232, v232
PW 160,192,228
s_waitcnt lgkmcnt(1)
v_exp_f32 v250, v250
PW 176,224,216
s_add_u32 m0, s60, 86912
CV 207,230,231
v_mul_f32 v234, s71, v234
PW 176,192,220
v_exp_f32 v228, v228
PW 172,220,216
v_exp_f32 v229, v229
PW 172,188,220
v_exp_f32 v234, v234
PW 168,216,216
s_waitcnt vmcnt(15) lgkmcnt(0)
CV 206,228,229
v_mul_f32 v233, s71, v233
PW 168,184,220
s_barrier
PW 164,212,216
L_s8_%=:
buffer_load_dwordx4 v237, s[48:51], s77 offen offset:128 lds
PW 164,180,220
v_exp_f32 v248, v248
QC 228,212,20
s_add_u32 m0, s60, 87808
RT 228,240,12288
QK 228,208,16
v_exp_f32 v235, v235
QK 228,204,12
CV 202,248,249
QK 228,200,8
CV 211,234,235
QK 228,196,4
v_exp_f32 v233, v233
QK 228,192,0
RT 224,240,0
CV 210,232,233
QC 232,192,24
buffer_load_dwordx4 v237, s[48:51], s77 offen offset:256 lds
QK 232,196,28
s_add_u32 m0, s60, 50944
v_exp_f32 v251, v251
QK 232,200,32
RT 226,240,6144
CV 203,250,251
QK 232,204,36
v_mul_f32 v228, s71, v228
v_mul_f32 v229, s71, v229
QK 232,208,40
RT 218,240,6160
v_mul_f32 v231, s71, v231
QK 232,212,44
v_exp_f32 v228, v228
QC 248,212,68
v_exp_f32 v229, v229
QK 248,208,64
L_c4_%=:
s_cbranch_execz L_c8_%=
v_mul_f32 v230, s71, v230
CV 212,228,229
QK 248,204,60
v_exp_f32 v231, v231
QK 248,200,56
v_exp_f32 v230, v230
QK 248,196,52
buffer_load_dwordx4 v237, s[48:51], s57 offen offset:256 lds
QK 248,192,48
CV 213,230,231
v_mul_f32 v234, s71, v234
QC 228,192,72
RT 230,240,18432
v_mul_f32 v232, s71, v232
QK 228,196,76
v_mul_f32 v233, s71, v233
QK 228,200,80
RQ 196,239,12288
QK 228,204,84
RT 220,240,12304
QK 228,208,88
RQ 208,238,14336
v_mul_f32 v249, s71, v249
QK 228,212,92
RT 216,240,16
v_mul_f32 v235, s71, v235
v_smfmac_f32_16x16x64_bf16 a[232:235], a[236:239], v[196:203], v236
s_waitcnt lgkmcnt(4)
PW 0,180,224
s_add_u32 m0, s61, 10112
RT 222,240,18448
v_mul_f32 v251, s71, v251
PW 0,196,228
RQ 200,238,13312
PW 4,184,224
v_exp_f32 v232, v232
PW 4,200,228
v_exp_f32 v249, v249
PW 8,188,224
v_exp_f32 v234, v234
PW 8,204,228
v_exp_f32 v235, v235
PW 12,192,224
RT 224,241,0
v_mul_f32 v248, s71, v248
PW 12,208,228
s_waitcnt lgkmcnt(1)
RT 226,241,6144
PW 28,192,216
RT 230,241,18432
v_mul_f32 v231, s71, v231
PW 28,208,220
RT 228,241,12288
CV 217,234,235
PW 24,188,216
v_mul_f32 v250, s71, v250
v_mul_f32 v230, s71, v230
PW 24,204,220
RQ 212,239,14336
PW 20,184,216
s_add_u32 s56, s62, 141312
v_exp_f32 v233, v233
PW 20,200,220
v_exp_f32 v231, v231
PW 16,180,216
RQ 192,238,12288
PW 16,196,220
s_waitcnt lgkmcnt(2)
CV 216,232,233
PW 32,180,224
v_mul_f32 v228, s71, v228
PW 32,196,228
RQ 204,239,13312
PW 36,184,224
v_exp_f32 v230, v230
PW 36,200,228
v_exp_f32 v248, v248
PW 40,188,224
v_exp_f32 v251, v251
PW 40,204,228
v_exp_f32 v228, v228
PW 44,192,224
s_waitcnt lgkmcnt(1)
s_add_u32 s58, s69, 12288
PW 44,208,228
buffer_load_dwordx4 v237, s[40:43], s58 offen offset:128 lds
QC 232,212,20
s_waitcnt lgkmcnt(0)
RT 224,240,1024
v_mul_f32 v229, s71, v229
QK 232,208,16
s_add_u32 m0, s60, 0
v_exp_f32 v250, v250
QK 232,204,12
CV 220,248,249
CV 225,230,231
QK 232,200,8
RT 220,241,12304
CV 221,250,251
QK 232,196,4
RT 218,241,6160
QK 232,192,0
RT 216,241,16
QC 248,192,24
s_waitcnt vmcnt(13)
v_exp_f32 v229, v229
QK 248,196,28
s_add_u32 s77, s62, 135168
RT 226,240,7168
QK 248,200,32
buffer_load_dwordx4 v237, s[44:47], s56 offen offset:0 lds
QK 248,204,36
v_mul_f32 v232, s71, v232
v_mul_f32 v233, s71, v233
QK 248,208,40
RT 230,240,19456
CV 224,228,229
QK 248,212,44
v_mul_f32 v234, s71, v234
v_mul_f32 v235, s71, v235
QC 228,212,68
v_exp_f32 v233, v233
QK 228,208,64
v_exp_f32 v232, v232
QK 228,204,60
v_exp_f32 v235, v235
QK 228,200,56
RQ 140,245,6144
CV 214,232,233
QK 228,196,52
s_add_u32 m0, s60, 896
v_exp_f32 v234, v234
QK 228,192,48
RT 222,241,18448
CV 215,234,235
QC 232,192,72
buffer_load_dwordx4 v237, s[44:47], s56 offen offset:128 lds
QK 232,196,76
RQ 136,244,6144
v_mul_f32 v249, s71, v249
QK 232,200,80
RT 228,240,13312
QK 232,204,84
s_add_u32 m0, s61, 8064
RQ 204,239,19456
v_mul_f32 v230, s71, v230
QK 232,208,88
RQ 208,238,20480
QK 232,212,92
RQ 200,238,19456
v_smfmac_f32_16x16x64_bf16 a[232:235], a[240:243], v[204:211], v236
s_waitcnt lgkmcnt(5)
RQ 212,239,20480
v_mul_f32 v248, s71, v248
PW 60,192,216
RQ 196,239,18432
v_mul_f32 v228, s71, v228
PW 60,208,220
v_exp_f32 v230, v230
PW 56,188,216
v_mul_f32 v231, s71, v231
PW 56,204,220
buffer_load_dwordx4 v237, s[40:43], s58 offen offset:3200 lds
PW 52,184,216
v_exp_f32 v231, v231
PW 52,200,220
CV 223,230,231
v_mul_f32 v234, s71, v234
PW 48,180,216
RT 218,240,7184
PW 48,196,220
RT 216,240,1040
PW 64,180,224
s_waitcnt lgkmcnt(2)
RT 222,240,19472
PW 64,196,228
RT 220,240,13328
v_mul_f32 v232, s71, v232
PW 68,184,224
RQ 192,238,18432
PW 68,200,228
v_mul_f32 v233, s71, v233
v_mul_f32 v235, s71, v235
PW 72,188,224
s_add_u32 m0, s60, 1792
v_exp_f32 v248, v248
PW 72,204,228
buffer_load_dwordx4 v237, s[44:47], s56 offen offset:256 lds
PW 76,192,224
v_exp_f32 v228, v228
PW 76,208,228
s_waitcnt lgkmcnt(1)
v_exp_f32 v232, v232
PW 92,192,216
v_exp_f32 v235, v235
PW 92,208,220
v_exp_f32 v234, v234
PW 88,188,216
s_add_u32 m0, s60, 50048
v_mul_f32 v250, s71, v250
v_mul_f32 v229, s71, v229
PW 88,204,220
v_exp_f32 v229, v229
PW 84,184,216
s_waitcnt vmcnt(15) lgkmcnt(0)
v_exp_f32 v233, v233
PW 84,200,220
s_barrier
PW 80,180,216
L_s9_%=:
buffer_load_dwordx4 v237, s[48:51], s57 offen offset:128 lds
PW 80,196,220
RT 224,241,13312
CV 222,228,229
QC 228,212,20
v_exp_f32 v249, v249
QK 228,208,16
s_add_u32 m0, s60, 49152
v_mul_f32 v251, s71, v251
CV 227,234,235
QK 228,204,12
QK 228,200,8
v_exp_f32 v250, v250
QK 228,196,4
RT 226,241,19456
CV 226,232,233
QK 228,192,0
RT 228,241,25600
QC 232,192,24
CV 218,248,249
QK 232,196,28
v_exp_f32 v251, v251
QK 232,200,32
buffer_load_dwordx4 v237, s[48:51], s57 offen offset:0 lds
QK 232,204,36
CV 219,250,251
v_mul_f32 v231, s71, v231
QK 232,208,40
s_add_u32 m0, s60, 61440
v_exp_f32 v231, v231
QK 232,212,44
v_mul_f32 v228, s71, v228
v_mul_f32 v229, s71, v229
QC 248,212,68
v_exp_f32 v228, v228
QK 248,208,64
v_exp_f32 v229, v229
QK 248,204,60
RT 220,241,25616
v_mul_f32 v230, s71, v230
QK 248,200,56
v_exp_f32 v230, v230
QK 248,196,52
s_add_u32 s58, s69, 12288
RT 230,241,31744
v_mul_f32 v235, s71, v235
QK 248,192,48
CV 180,228,229
CV 181,230,231
QC 228,192,72
buffer_load_dwordx4 v237, s[48:51], s77 offen offset:0 lds
QK 228,196,76
RT 222,241,31760
QK 228,200,80
RQ 200,238,25600
QK 228,204,84
v_mul_f32 v250, s71, v250
QK 228,208,88
RT 216,241,13328
QK 228,212,92
s_add_u32 s56, s62, 153600
)"
R"(
v_smfmac_f32_16x16x64_bf16 a[232:235], a[236:239], v[212:219], v236
s_waitcnt lgkmcnt(3)
v_mul_f32 v251, s71, v251
PW 96,196,224
RT 218,241,19472
PW 96,212,228
s_add_u32 m0, s61, 12032
RQ 204,239,25600
v_mul_f32 v233, s71, v233
PW 100,200,224
buffer_load_dwordx4 v237, s[40:43], s58 offen offset:256 lds
PW 100,216,228
v_mul_f32 v231, s71, v231
PW 104,204,224
v_exp_f32 v233, v233
PW 104,220,228
v_exp_f32 v250, v250
PW 108,208,224
s_add_u32 m0, s60, 12288
RT 226,240,20480
PW 108,224,228
s_waitcnt lgkmcnt(1)
RT 228,240,26624
v_mul_f32 v234, s71, v234
PW 112,208,216
RT 224,240,14336
v_mul_f32 v232, s71, v232
PW 112,224,220
RT 230,240,32768
v_mul_f32 v229, s71, v229
v_mfma_f32_16x16x32_bf16 a[252:255], v[204:207], a[216:219], a[252:255]
v_exp_f32 v232, v232
v_mfma_f32_16x16x32_bf16 a[252:255], v[220:223], a[220:223], a[252:255]
v_exp_f32 v234, v234
v_mfma_f32_16x16x32_bf16 a[248:251], v[200:203], a[216:219], a[248:251]
buffer_load_dwordx4 v237, s[44:47], s56 offen offset:0 lds
v_mfma_f32_16x16x32_bf16 a[248:251], v[216:219], a[220:223], a[248:251]
RQ 212,239,26624
v_mul_f32 v248, s71, v248
v_mfma_f32_16x16x32_bf16 a[244:247], v[196:199], a[216:219], a[244:247]
RQ 208,238,26624
v_mfma_f32_16x16x32_bf16 a[244:247], v[212:215], a[220:223], a[244:247]
s_waitcnt lgkmcnt(2)
v_exp_f32 v231, v231
PW 116,196,224
RQ 196,239,24576
CV 184,232,233
PW 116,212,228
s_add_u32 m0, s60, 13184
v_exp_f32 v235, v235
PW 120,200,224
v_mul_f32 v249, s71, v249
PW 120,216,228
RQ 192,238,24576
v_mul_f32 v230, s71, v230
PW 124,204,224
RT 216,240,14352
CV 185,234,235
PW 124,220,228
v_exp_f32 v229, v229
PW 128,208,224
buffer_load_dwordx4 v237, s[44:47], s56 offen offset:128 lds
PW 128,224,228
s_waitcnt lgkmcnt(2)
RT 222,240,32784
v_mul_f32 v228, s71, v228
QC 232,212,20
s_add_u32 m0, s61, 9984
v_exp_f32 v251, v251
QK 232,208,16
RT 220,240,26640
CV 189,250,251
QK 232,204,12
v_exp_f32 v249, v249
QK 232,200,8
v_exp_f32 v230, v230
QK 232,196,4
s_waitcnt lgkmcnt(2)
v_exp_f32 v248, v248
QK 232,192,0
CV 188,248,249
QC 248,192,24
s_waitcnt vmcnt(15)
v_exp_f32 v228, v228
QK 248,196,28
RT 218,240,20496
CV 193,230,231
QK 248,200,32
buffer_load_dwordx4 v237, s[40:43], s58 offen offset:3328 lds
QK 248,204,36
CV 192,228,229
v_mul_f32 v232, s71, v232
QK 248,208,40
v_exp_f32 v232, v232
QK 248,212,44
v_mul_f32 v235, s71, v235
QC 228,212,68
v_exp_f32 v235, v235
QK 228,208,64
v_mul_f32 v233, s71, v233
v_mul_f32 v234, s71, v234
QK 228,204,60
v_exp_f32 v233, v233
QK 228,200,56
v_exp_f32 v234, v234
QK 228,196,52
CV 183,234,235
v_mul_f32 v249, s71, v249
QK 228,192,48
s_add_u32 m0, s60, 14080
RQ 144,244,8192
CV 182,232,233
QC 232,192,72
buffer_load_dwordx4 v237, s[44:47], s56 offen offset:256 lds
QK 232,196,76
RT 228,241,26624
QK 232,200,80
s_add_u32 s56, s62, 165888
RT 226,241,20480
QK 232,204,84
RT 230,241,32768
QK 232,208,88
RQ 196,239,30720
v_mul_f32 v228, s71, v228
QK 232,212,92
v_mul_f32 v250, s71, v250
v_mul_f32 v230, s71, v230
v_smfmac_f32_16x16x64_bf16 a[232:235], a[240:243], v[220:227], v236
s_waitcnt lgkmcnt(4)
RT 224,241,14336
v_mul_f32 v229, s71, v229
PW 144,208,216
v_exp_f32 v229, v229
PW 144,224,220
RQ 148,245,8192
v_mul_f32 v248, s71, v248
PW 140,204,216
RQ 204,239,31744
v_mul_f32 v251, s71, v251
PW 140,220,220
RQ 200,238,31744
PW 136,200,216
v_exp_f32 v228, v228
PW 136,216,220
v_mul_f32 v232, s71, v232
v_mul_f32 v233, s71, v233
PW 132,196,216
RT 218,241,20496
PW 132,212,220
s_waitcnt lgkmcnt(3)
RT 216,241,14352
v_mul_f32 v234, s71, v234
PW 148,196,224
RT 222,241,32784
v_mul_f32 v235, s71, v235
PW 148,212,228
RT 220,241,26640
PW 152,200,224
RQ 192,238,30720
PW 152,216,228
RQ 212,239,32768
CV 190,228,229
PW 156,204,224
v_exp_f32 v233, v233
PW 156,220,228
RQ 208,238,32768
v_mul_f32 v231, s71, v231
PW 160,208,224
v_exp_f32 v248, v248
PW 160,224,228
s_waitcnt lgkmcnt(3)
v_exp_f32 v230, v230
PW 176,208,216
v_exp_f32 v232, v232
PW 176,224,220
v_exp_f32 v231, v231
PW 172,204,216
v_exp_f32 v235, v235
PW 172,220,220
v_exp_f32 v250, v250
PW 168,200,216
s_waitcnt vmcnt(15) lgkmcnt(0)
s_add_u32 m0, s60, 62336
CV 194,232,233
PW 168,216,220
s_barrier
PW 164,196,216
L_s10_%=:
buffer_load_dwordx4 v237, s[48:51], s77 offen offset:128 lds
PW 164,212,220
s_add_u32 m0, s60, 63232
CV 191,230,231
QC 228,212,20
v_exp_f32 v249, v249
QK 228,208,16
v_exp_f32 v234, v234
QK 228,204,12
s_add_u32 s57, s62, 147456
RT 224,240,24576
CV 186,248,249
QK 228,200,8
v_exp_f32 v251, v251
QK 228,196,4
RT 218,240,30736
CV 195,234,235
QK 228,192,0
RT 228,240,36864
QC 232,192,24
buffer_load_dwordx4 v237, s[48:51], s77 offen offset:256 lds
QK 232,196,28
RT 226,240,30720
CV 187,250,251
QK 232,200,32
RT 220,240,36880
QK 232,204,36
s_add_u32 m0, s60, 75520
RT 230,240,43008
QK 232,208,40
s_add_u32 s58, s69, 18432
v_mul_f32 v229, s71, v229
v_mul_f32 v231, s71, v231
QK 232,212,44
v_mul_f32 v228, s71, v228
v_mul_f32 v230, s71, v230
QC 248,212,68
v_exp_f32 v230, v230
QK 248,208,64
v_exp_f32 v231, v231
QK 248,204,60
v_exp_f32 v228, v228
QK 248,200,56
buffer_load_dwordx4 v237, s[48:51], s57 offen offset:256 lds
QK 248,196,52
v_exp_f32 v229, v229
QK 248,192,48
s_add_u32 m0, s61, 14336
CV 196,228,229
CV 197,230,231
QC 228,192,72
v_mul_f32 v235, s71, v235
QK 228,196,76
RT 222,240,43024
QK 228,200,80
v_exp_f32 v235, v235
QK 228,204,84
RT 216,240,24592
QK 228,208,88
RQ 196,239,36864
v_mul_f32 v248, s71, v248
QK 228,212,92
v_mul_f32 v232, s71, v232
v_mul_f32 v250, s71, v250
v_smfmac_f32_16x16x64_bf16 a[232:235], a[236:239], v[180:187], v236
s_waitcnt lgkmcnt(3)
v_mul_f32 v249, s71, v249
PW 0,212,224
buffer_load_dwordx4 v237, s[40:43], s58 offen offset:0 lds
PW 0,180,228
RQ 212,239,38912
PW 4,216,224
v_mul_f32 v234, s71, v234
PW 4,184,228
PW 8,220,224
v_exp_f32 v234, v234
PW 8,188,228
RQ 208,238,38912
PW 12,224,224
RT 226,241,30720
PW 12,192,228
s_waitcnt lgkmcnt(2)
RT 228,241,36864
v_mul_f32 v233, s71, v233
PW 28,224,216
s_add_u32 m0, s60, 24576
RT 224,241,24576
CV 201,234,235
PW 28,192,220
RT 230,241,43008
PW 24,220,216
buffer_load_dwordx4 v237, s[44:47], s56 offen offset:0 lds
PW 24,188,220
v_exp_f32 v249, v249
PW 20,216,216
RQ 192,238,36864
v_mul_f32 v230, s71, v230
PW 20,184,220
RQ 200,238,37888
v_mul_f32 v231, s71, v231
PW 16,212,216
v_exp_f32 v230, v230
PW 16,180,220
s_waitcnt lgkmcnt(2)
RT 216,241,24592
v_mul_f32 v229, s71, v229
PW 32,212,224
v_exp_f32 v233, v233
PW 32,180,228
RQ 204,239,37888
v_mul_f32 v251, s71, v251
PW 36,216,224
v_exp_f32 v250, v250
PW 36,184,228
v_exp_f32 v231, v231
PW 40,220,224
v_exp_f32 v232, v232
PW 40,188,228
RT 220,241,36880
PW 44,224,224
v_exp_f32 v229, v229
PW 44,192,228
CV 200,232,233
QC 232,212,20
v_exp_f32 v248, v248
QK 232,208,16
s_waitcnt lgkmcnt(1)
CV 204,248,249
QK 232,204,12
v_exp_f32 v251, v251
QK 232,200,8
RT 228,240,37888
QK 232,196,4
RT 218,241,30736
v_mul_f32 v228, s71, v228
QK 232,192,0
RT 224,240,25600
CV 205,250,251
QC 248,192,24
s_waitcnt vmcnt(14)
v_exp_f32 v228, v228
QK 248,196,28
RT 222,241,43024
CV 208,228,229
QK 248,200,32
CV 209,230,231
L_c5_%=:
s_cbranch_execz L_c9_%=
QK 248,204,36
v_mul_f32 v234, s71, v234
v_mul_f32 v235, s71, v235
QK 248,208,40
v_exp_f32 v235, v235
QK 248,212,44
s_add_u32 m0, s60, 25472
v_exp_f32 v234, v234
QC 228,212,68
v_mul_f32 v233, s71, v233
QK 228,208,64
v_mul_f32 v232, s71, v232
QK 228,204,60
RQ 156,245,10240
CV 199,234,235
QK 228,200,56
v_exp_f32 v232, v232
QK 228,196,52
v_exp_f32 v233, v233
QK 228,192,48
RQ 152,244,10240
CV 198,232,233
QC 232,192,72
QK 232,196,76
buffer_load_dwordx4 v237, s[44:47], s56 offen offset:128 lds
QK 232,200,80
RQ 200,238,44032
v_mul_f32 v248, s71, v248
QK 232,204,84
RT 226,240,31744
v_mul_f32 v249, s71, v249
QK 232,208,88
RQ 192,238,43008
QK 232,212,92
RT 230,240,44032
v_smfmac_f32_16x16x64_bf16 a[232:235], a[240:243], v[188:195], v236
s_waitcnt lgkmcnt(4)
s_add_u32 m0, s61, 12288
RQ 204,239,44032
PW 60,224,216
v_mul_f32 v250, s71, v250
v_mul_f32 v231, s71, v231
PW 60,192,220
RQ 212,239,45056
v_mul_f32 v229, s71, v229
PW 56,220,216
RQ 208,238,45056
PW 56,188,220
buffer_load_dwordx4 v237, s[40:43], s58 offen offset:3072 lds
PW 52,216,216
v_mul_f32 v228, s71, v228
v_mul_f32 v235, s71, v235
PW 52,184,220
v_exp_f32 v229, v229
PW 48,212,216
RT 216,240,25616
v_mul_f32 v251, s71, v251
PW 48,180,220
s_waitcnt lgkmcnt(2)
RT 218,240,31760
v_mul_f32 v234, s71, v234
PW 64,212,224
RT 220,240,37904
v_mul_f32 v230, s71, v230
)"
R"(
PW 64,180,228
RT 222,240,44048
PW 68,216,224
s_add_u32 m0, s60, 26368
v_exp_f32 v231, v231
PW 68,184,228
RQ 196,239,43008
v_mul_f32 v233, s71, v233
PW 72,220,224
buffer_load_dwordx4 v237, s[44:47], s56 offen offset:256 lds
PW 72,188,228
v_exp_f32 v228, v228
PW 76,224,224
v_exp_f32 v248, v248
PW 76,192,228
s_waitcnt lgkmcnt(1)
v_exp_f32 v234, v234
PW 92,224,216
v_exp_f32 v230, v230
PW 92,192,220
s_add_u32 m0, s60, 74624
CV 207,230,231
PW 88,220,216
CV 206,228,229
v_mul_f32 v232, s71, v232
PW 88,188,220
v_exp_f32 v251, v251
PW 84,216,216
s_waitcnt vmcnt(15) lgkmcnt(0)
v_exp_f32 v235, v235
PW 84,184,220
s_barrier
PW 80,212,216
L_s11_%=:
buffer_load_dwordx4 v237, s[48:51], s57 offen offset:128 lds
PW 80,180,220
CV 211,234,235
QC 228,212,20
v_exp_f32 v232, v232
QK 228,208,16
RT 224,241,37888
QK 228,204,12
s_add_u32 m0, s60, 73728
v_exp_f32 v233, v233
QK 228,200,8
CV 210,232,233
QK 228,196,4
v_exp_f32 v250, v250
QK 228,192,0
RT 218,241,44048
CV 203,250,251
QC 232,192,24
buffer_load_dwordx4 v237, s[48:51], s57 offen offset:0 lds
QK 232,196,28
v_exp_f32 v249, v249
QK 232,200,32
RT 228,241,1024
CV 202,248,249
QK 232,204,36
v_mul_f32 v228, s71, v228
v_mul_f32 v231, s71, v231
QK 232,208,40
RT 220,241,1040
QK 232,212,44
RT 226,241,44032
QC 248,212,68
v_exp_f32 v231, v231
QK 248,208,64
v_mul_f32 v229, s71, v229
v_mul_f32 v230, s71, v230
QK 248,204,60
v_exp_f32 v228, v228
QK 248,200,56
v_exp_f32 v230, v230
QK 248,196,52
s_add_u32 s77, s62, 159744
v_exp_f32 v229, v229
QK 248,192,48
CV 212,228,229
CV 213,230,231
QC 228,192,72
RT 230,241,7168
v_mul_f32 v233, s71, v233
QK 228,196,76
RT 216,241,37904
QK 228,200,80
RT 222,241,7184
QK 228,204,84
RQ 196,239,0
QK 228,208,88
v_mul_f32 v232, s71, v232
v_mul_f32 v250, s71, v250
QK 228,212,92
v_exp_f32 v250, v250
v_smfmac_f32_16x16x64_bf16 a[232:235], a[236:239], v[196:203], v236
s_waitcnt lgkmcnt(3)
RQ 192,238,0
PW 96,180,224
PW 96,196,228
v_mul_f32 v234, s71, v234
v_mul_f32 v248, s71, v248
PW 100,184,224
RQ 212,239,2048
v_mul_f32 v235, s71, v235
PW 100,200,228
s_add_u32 m0, s60, 86016
RQ 208,238,2048
PW 104,188,224
v_mul_f32 v251, s71, v251
PW 104,204,228
PW 108,192,224
RT 226,240,45056
v_mul_f32 v249, s71, v249
PW 108,208,228
s_waitcnt lgkmcnt(3)
RT 224,240,38912
PW 112,192,216
RT 228,240,2048
v_mul_f32 v229, s71, v229
PW 112,208,220
RT 230,240,8192
v_mul_f32 v230, s71, v230
v_mfma_f32_16x16x32_bf16 a[252:255], v[188:191], a[216:219], a[252:255]
RQ 200,238,1024
v_mul_f32 v228, s71, v228
v_mfma_f32_16x16x32_bf16 a[252:255], v[204:207], a[220:223], a[252:255]
v_mul_f32 v231, s71, v231
v_mfma_f32_16x16x32_bf16 a[248:251], v[184:187], a[216:219], a[248:251]
buffer_load_dwordx4 v237, s[48:51], s77 offen offset:0 lds
v_mfma_f32_16x16x32_bf16 a[248:251], v[200:203], a[220:223], a[248:251]
v_exp_f32 v248, v248
v_mfma_f32_16x16x32_bf16 a[244:247], v[180:183], a[216:219], a[244:247]
v_exp_f32 v232, v232
v_mfma_f32_16x16x32_bf16 a[244:247], v[196:199], a[220:223], a[244:247]
s_waitcnt lgkmcnt(1)
v_exp_f32 v233, v233
PW 116,180,224
RQ 204,239,1024
CV 216,232,233
PW 116,196,228
v_exp_f32 v228, v228
PW 120,184,224
s_add_u32 m0, s61, 3968
v_exp_f32 v234, v234
PW 120,200,228
v_exp_f32 v235, v235
PW 124,188,224
s_add_u32 s58, s69, 18432
v_exp_f32 v251, v251
PW 124,204,228
s_add_u32 s56, s62, 178176
v_exp_f32 v249, v249
PW 128,192,224
buffer_load_dwordx4 v237, s[40:43], s58 offen offset:128 lds
PW 128,208,228
RT 228,241,2048
CV 217,234,235
QC 232,212,20
v_exp_f32 v231, v231
QK 232,208,16
s_waitcnt lgkmcnt(1)
v_exp_f32 v230, v230
QK 232,204,12
RT 220,240,2064
CV 220,248,249
QK 232,200,8
v_exp_f32 v229, v229
QK 232,196,4
RT 222,240,8208
CV 221,250,251
QK 232,192,0
CV 225,230,231
QC 248,192,24
s_waitcnt vmcnt(13)
RT 216,240,38928
QK 248,196,28
s_add_u32 m0, s60, 36864
CV 224,228,229
QK 248,200,32
buffer_load_dwordx4 v237, s[44:47], s56 offen offset:0 lds
QK 248,204,36
v_mul_f32 v232, s71, v232
v_mul_f32 v234, s71, v234
QK 248,208,40
v_mul_f32 v233, s71, v233
QK 248,212,44
s_add_u32 m0, s60, 37760
v_exp_f32 v234, v234
QC 228,212,68
v_exp_f32 v232, v232
QK 228,208,64
v_exp_f32 v233, v233
QK 228,204,60
RT 218,240,45072
CV 214,232,233
QK 228,200,56
RT 224,241,38912
v_mul_f32 v235, s71, v235
QK 228,196,52
v_exp_f32 v235, v235
QK 228,192,48
RQ 164,245,12288
CV 215,234,235
QC 232,192,72
buffer_load_dwordx4 v237, s[44:47], s56 offen offset:128 lds
QK 232,196,76
RT 226,241,45056
QK 232,200,80
RQ 196,239,6144
QK 232,204,84
RQ 200,238,7168
v_mul_f32 v231, s71, v231
QK 232,208,88
RT 230,241,8192
v_mul_f32 v249, s71, v249
QK 232,212,92
s_add_u32 m0, s61, 1920
v_mul_f32 v230, s71, v230
v_smfmac_f32_16x16x64_bf16 a[232:235], a[240:243], v[204:211], v236
s_waitcnt lgkmcnt(4)
v_exp_f32 v230, v230
PW 144,192,216
RQ 160,244,12288
v_mul_f32 v228, s71, v228
PW 144,208,220
v_exp_f32 v231, v231
PW 140,188,216
v_exp_f32 v228, v228
PW 140,204,220
buffer_load_dwordx4 v237, s[40:43], s58 offen offset:3200 lds
PW 136,184,216
s_add_u32 m0, s60, 38656
RQ 192,238,6144
PW 136,200,220
RQ 208,238,8192
PW 132,180,216
RT 218,241,45072
v_mul_f32 v233, s71, v233
PW 132,196,220
s_waitcnt lgkmcnt(3)
RT 220,241,2064
PW 148,180,224
RT 216,241,38928
CV 223,230,231
PW 148,196,228
RT 222,241,8208
v_mul_f32 v250, s71, v250
PW 152,184,224
RQ 204,239,7168
PW 152,200,228
s_add_u32 s58, s69, 18432
RQ 212,239,8192
v_mul_f32 v229, s71, v229
PW 156,188,224
v_mul_f32 v251, s71, v251
PW 156,204,228
buffer_load_dwordx4 v237, s[44:47], s56 offen offset:256 lds
PW 160,192,224
s_add_u32 s57, s62, 172032
v_exp_f32 v229, v229
PW 160,208,228
s_waitcnt lgkmcnt(2)
s_add_u32 m0, s60, 86912
v_mul_f32 v235, s71, v235
PW 176,192,216
v_mul_f32 v248, s71, v248
CV 222,228,229
PW 176,208,220
v_mul_f32 v232, s71, v232
v_mul_f32 v234, s71, v234
PW 172,188,216
v_exp_f32 v235, v235
PW 172,204,220
v_exp_f32 v250, v250
PW 168,184,216
s_waitcnt vmcnt(15) lgkmcnt(0)
v_exp_f32 v234, v234
PW 168,200,220
s_barrier
PW 164,180,216
L_s12_%=:
v_exp_f32 v233, v233
PW 164,196,220
buffer_load_dwordx4 v237, s[48:51], s77 offen offset:128 lds
QC 228,212,20
v_exp_f32 v249, v249
QK 228,208,16
v_exp_f32 v251, v251
QK 228,204,12
v_exp_f32 v248, v248
QK 228,200,8
v_exp_f32 v232, v232
QK 228,196,4
RT 228,240,12288
CV 227,234,235
QK 228,192,0
CV 226,232,233
QC 232,192,24
RT 216,240,16
CV 219,250,251
QK 232,196,28
RT 224,240,0
CV 218,248,249
QK 232,200,32
RT 226,240,6144
QK 232,204,36
v_mul_f32 v229, s71, v229
v_mul_f32 v230, s71, v230
QK 232,208,40
s_add_u32 m0, s60, 87808
v_mul_f32 v231, s71, v231
QK 232,212,44
RT 230,240,18432
v_mul_f32 v228, s71, v228
QC 248,212,68
v_exp_f32 v228, v228
QK 248,208,64
v_exp_f32 v230, v230
QK 248,204,60
v_exp_f32 v231, v231
QK 248,200,56
buffer_load_dwordx4 v237, s[48:51], s77 offen offset:256 lds
QK 248,196,52
v_exp_f32 v229, v229
QK 248,192,48
CV 180,228,229
CV 181,230,231
QC 228,192,72
RT 222,240,18448
v_mul_f32 v232, s71, v232
QK 228,196,76
s_add_u32 m0, s60, 50944
s_add_u32 s77, s62, 184320
RT 220,240,12304
QK 228,200,80
v_exp_f32 v232, v232
QK 228,204,84
RQ 200,238,13312
QK 228,208,88
v_mul_f32 v233, s71, v233
QK 228,212,92
s_waitcnt lgkmcnt(3)
RQ 196,239,12288
v_mul_f32 v251, s71, v251
v_smfmac_f32_16x16x64_bf16 a[232:235], a[236:239], v[212:219], v236
s_mov_b32 s56, s65
RT 218,240,6160
v_mul_f32 v234, s71, v234
PW 0,196,224
buffer_load_dwordx4 v237, s[48:51], s57 offen offset:256 lds
PW 0,212,228
RQ 212,239,14336
v_mul_f32 v248, s71, v248
PW 4,200,224
v_exp_f32 v248, v248
PW 4,216,228
s_add_u32 m0, s61, 5888
RQ 204,239,13312
PW 8,204,224
RQ 192,238,12288
v_mul_f32 v250, s71, v250
PW 8,220,228
v_exp_f32 v251, v251
PW 12,208,224
RT 226,241,6144
PW 12,224,228
s_waitcnt lgkmcnt(3)
RT 228,241,12288
PW 28,208,216
RT 224,241,0
PW 28,224,220
RT 230,241,18432
v_mul_f32 v249, s71, v249
PW 24,204,216
buffer_load_dwordx4 v237, s[40:43], s58 offen offset:256 lds
PW 24,220,220
v_mul_f32 v235, s71, v235
v_mul_f32 v231, s71, v231
PW 20,200,216
v_exp_f32 v234, v234
PW 20,216,220
v_mul_f32 v230, s71, v230
PW 16,196,216
v_exp_f32 v233, v233
PW 16,212,220
s_waitcnt lgkmcnt(0)
v_exp_f32 v235, v235
PW 32,196,224
RQ 208,238,14336
CV 185,234,235
PW 32,212,228
s_add_u32 m0, s60, 0
RT 220,241,12304
v_mul_f32 v229, s71, v229
PW 36,200,224
buffer_load_dwordx4 v237, s[44:47], s56 offen offset:0 lds
PW 36,216,228
s_add_u32 m0, s60, 896
v_exp_f32 v249, v249
PW 40,204,224
)"
R"(
v_exp_f32 v250, v250
PW 40,220,228
v_exp_f32 v231, v231
PW 44,208,224
CV 184,232,233
v_mul_f32 v228, s71, v228
PW 44,224,228
v_exp_f32 v229, v229
QC 232,212,20
s_waitcnt lgkmcnt(0)
CV 189,250,251
QK 232,208,16
CV 188,248,249
QK 232,204,12
buffer_load_dwordx4 v237, s[44:47], s56 offen offset:128 lds
QK 232,200,8
RT 216,241,16
QK 232,196,4
v_exp_f32 v230, v230
QK 232,192,0
v_exp_f32 v228, v228
QC 248,192,24
s_waitcnt vmcnt(15)
RT 218,241,6160
CV 193,230,231
QK 248,196,28
RT 226,240,7168
QK 248,200,32
RT 230,240,19456
CV 192,228,229
QK 248,204,36
s_add_u32 m0, s61, 3840
RQ 168,244,14336
v_mul_f32 v234, s71, v234
QK 248,208,40
v_mul_f32 v232, s71, v232
v_mul_f32 v235, s71, v235
QK 248,212,44
buffer_load_dwordx4 v237, s[40:43], s58 offen offset:3328 lds
QC 228,212,68
RT 222,241,18448
v_mul_f32 v233, s71, v233
QK 228,208,64
v_exp_f32 v235, v235
QK 228,204,60
s_add_u32 m0, s60, 1792
v_exp_f32 v233, v233
QK 228,200,56
v_exp_f32 v232, v232
QK 228,196,52
v_exp_f32 v234, v234
QK 228,192,48
CV 182,232,233
CV 183,234,235
QC 232,192,72
QK 232,196,76
RT 228,240,13312
QK 232,200,80
RQ 172,245,14336
QK 232,204,84
RT 224,240,1024
QK 232,208,88
RQ 204,239,19456
QK 232,212,92
v_mul_f32 v248, s71, v248
v_mul_f32 v230, s71, v230
v_smfmac_f32_16x16x64_bf16 a[232:235], a[240:243], v[220:227], v236
s_waitcnt lgkmcnt(4)
RQ 200,238,19456
v_mul_f32 v250, s71, v250
PW 60,208,216
RQ 208,238,20480
v_mul_f32 v249, s71, v249
PW 60,224,220
v_exp_f32 v230, v230
PW 56,204,216
RQ 192,238,18432
v_mul_f32 v229, s71, v229
PW 56,220,220
RQ 212,239,20480
v_mul_f32 v228, s71, v228
PW 52,200,216
v_exp_f32 v229, v229
PW 52,216,220
RQ 196,239,18432
v_mul_f32 v233, s71, v233
PW 48,196,216
RT 218,240,7184
v_mul_f32 v251, s71, v251
PW 48,212,220
s_waitcnt lgkmcnt(4)
RT 220,240,13328
v_mul_f32 v231, s71, v231
PW 64,196,224
RT 216,240,1040
PW 64,212,228
RT 222,240,19472
v_mul_f32 v235, s71, v235
PW 68,200,224
s_add_u32 s58, s70, 0
v_exp_f32 v228, v228
PW 68,216,228
v_mul_f32 v232, s71, v232
PW 72,204,224
buffer_load_dwordx4 v237, s[44:47], s56 offen offset:256 lds
PW 72,220,228
v_exp_f32 v250, v250
PW 76,208,224
v_exp_f32 v251, v251
PW 76,224,228
s_waitcnt lgkmcnt(0)
CV 190,228,229
v_mul_f32 v234, s71, v234
PW 92,208,216
v_exp_f32 v232, v232
PW 92,224,220
v_exp_f32 v234, v234
PW 88,204,216
v_exp_f32 v231, v231
PW 88,220,220
v_exp_f32 v235, v235
PW 84,200,216
s_waitcnt vmcnt(15)
s_add_u32 m0, s60, 50048
CV 191,230,231
PW 84,216,220
s_barrier
PW 80,196,216
L_s13_%=:
buffer_load_dwordx4 v237, s[48:51], s57 offen offset:128 lds
PW 80,212,220
RT 226,241,19456
CV 195,234,235
QC 228,212,20
v_exp_f32 v233, v233
QK 228,208,16
s_add_u32 m0, s60, 49152
v_exp_f32 v248, v248
QK 228,204,12
RT 218,241,19472
CV 194,232,233
QK 228,200,8
v_exp_f32 v249, v249
QK 228,196,4
QK 228,192,0
L_c6_%=:
s_cbranch_execz L_c10_%=
RT 216,241,13328
QC 232,192,24
RT 228,241,25600
QK 232,196,28
CV 186,248,249
CV 187,250,251
QK 232,200,32
buffer_load_dwordx4 v237, s[48:51], s57 offen offset:0 lds
QK 232,204,36
v_mul_f32 v228, s71, v228
v_mul_f32 v230, s71, v230
QK 232,208,40
v_exp_f32 v228, v228
QK 232,212,44
v_mul_f32 v229, s71, v229
QC 248,212,68
v_exp_f32 v230, v230
QK 248,208,64
RT 230,241,31744
v_mul_f32 v231, s71, v231
QK 248,204,60
v_exp_f32 v231, v231
QK 248,200,56
s_add_u32 m0, s60, 61440
v_exp_f32 v229, v229
QK 248,196,52
RT 224,241,13312
CV 196,228,229
QK 248,192,48
CV 197,230,231
v_mul_f32 v232, s71, v232
QC 228,192,72
buffer_load_dwordx4 v237, s[48:51], s77 offen offset:0 lds
QK 228,196,76
RT 222,241,31760
QK 228,200,80
s_add_u32 s56, s63, 6144
RT 220,241,25616
v_mul_f32 v235, s71, v235
QK 228,204,84
v_exp_f32 v235, v235
QK 228,208,88
RQ 204,239,25600
QK 228,212,92
RQ 212,239,26624
v_smfmac_f32_16x16x64_bf16 a[232:235], a[236:239], v[180:187], v236
s_waitcnt lgkmcnt(4)
v_mul_f32 v233, s71, v233
PW 96,212,224
v_exp_f32 v232, v232
PW 96,180,228
s_add_u32 m0, s61, 8192
v_exp_f32 v233, v233
PW 100,216,224
RQ 200,238,25600
v_mul_f32 v251, s71, v251
PW 100,184,228
buffer_load_dwordx4 v237, s[40:43], s58 offen offset:0 lds
PW 104,220,224
RQ 208,238,26624
v_mul_f32 v234, s71, v234
PW 104,188,228
v_exp_f32 v234, v234
PW 108,224,224
RT 224,240,14336
v_mul_f32 v250, s71, v250
PW 108,192,228
s_add_u32 m0, s60, 12288
RT 226,240,20480
v_mul_f32 v248, s71, v248
PW 112,224,216
s_waitcnt lgkmcnt(3)
RT 228,240,26624
PW 112,192,220
RT 230,240,32768
v_mfma_f32_16x16x32_bf16 a[252:255], v[220:223], a[216:219], a[252:255]
v_exp_f32 v250, v250
v_mfma_f32_16x16x32_bf16 a[252:255], v[188:191], a[220:223], a[252:255]
buffer_load_dwordx4 v237, s[44:47], s56 offen offset:0 lds
v_mfma_f32_16x16x32_bf16 a[248:251], v[216:219], a[216:219], a[248:251]
v_mul_f32 v231, s71, v231
v_mfma_f32_16x16x32_bf16 a[248:251], v[184:187], a[220:223], a[248:251]
RQ 196,239,24576
CV 200,232,233
v_mfma_f32_16x16x32_bf16 a[244:247], v[212:215], a[216:219], a[244:247]
s_waitcnt lgkmcnt(2)
RT 216,240,14352
v_mul_f32 v230, s71, v230
v_mfma_f32_16x16x32_bf16 a[244:247], v[180:183], a[220:223], a[244:247]
s_add_u32 s57, s63, 0
v_exp_f32 v251, v251
PW 116,212,224
s_waitcnt lgkmcnt(2)
PW 116,180,228
v_mul_f32 v249, s71, v249
v_mul_f32 v228, s71, v228
PW 120,216,224
RQ 192,238,24576
CV 201,234,235
PW 120,184,228
CV 205,250,251
PW 124,220,224
s_add_u32 m0, s60, 13184
v_exp_f32 v231, v231
PW 124,188,228
buffer_load_dwordx4 v237, s[44:47], s56 offen offset:128 lds
PW 128,224,224
RT 224,241,14336
v_mul_f32 v229, s71, v229
PW 128,192,228
v_exp_f32 v249, v249
QC 232,212,20
v_exp_f32 v230, v230
QK 232,208,16
v_exp_f32 v248, v248
QK 232,204,12
RT 222,240,32784
QK 232,200,8
s_waitcnt lgkmcnt(2)
s_add_u32 m0, s61, 6144
CV 204,248,249
QK 232,196,4
RT 228,241,26624
QK 232,192,0
v_exp_f32 v229, v229
QC 248,192,24
s_waitcnt vmcnt(15)
CV 209,230,231
QK 248,196,28
buffer_load_dwordx4 v237, s[40:43], s58 offen offset:3072 lds
QK 248,200,32
v_exp_f32 v228, v228
QK 248,204,36
v_mul_f32 v232, s71, v232
v_mul_f32 v235, s71, v235
QK 248,208,40
RT 230,241,32768
CV 208,228,229
QK 248,212,44
v_mul_f32 v233, s71, v233
v_mul_f32 v234, s71, v234
QC 228,212,68
s_add_u32 m0, s60, 14080
v_exp_f32 v234, v234
QK 228,208,64
v_exp_f32 v235, v235
QK 228,204,60
v_exp_f32 v233, v233
QK 228,200,56
v_exp_f32 v232, v232
QK 228,196,52
RT 218,240,20496
CV 199,234,235
QK 228,192,48
RT 220,240,26640
CV 198,232,233
QC 232,192,72
QK 232,196,76
RT 226,241,20480
QK 232,200,80
RQ 176,244,4096
QK 232,204,84
RQ 180,245,4096
v_mul_f32 v249, s71, v249
QK 232,208,88
RQ 208,238,32768
v_mul_f32 v251, s71, v251
QK 232,212,92
RQ 192,238,30720
v_mul_f32 v228, s71, v228
v_smfmac_f32_16x16x64_bf16 a[232:235], a[240:243], v[188:195], v236
s_waitcnt lgkmcnt(5)
v_mul_f32 v230, s71, v230
PW 144,224,216
v_exp_f32 v228, v228
PW 144,192,220
RQ 204,239,31744
v_mul_f32 v250, s71, v250
PW 140,220,216
RQ 200,238,31744
v_mul_f32 v248, s71, v248
PW 140,188,220
v_exp_f32 v251, v251
PW 136,216,216
RQ 212,239,32768
PW 136,184,220
v_exp_f32 v249, v249
PW 132,212,216
RT 218,241,20496
v_mul_f32 v231, s71, v231
PW 132,180,220
s_waitcnt lgkmcnt(3)
RT 216,241,14352
v_mul_f32 v234, s71, v234
PW 148,212,224
RT 222,241,32784
v_mul_f32 v232, s71, v232
PW 148,180,228
RT 220,241,26640
v_mul_f32 v233, s71, v233
PW 152,216,224
s_add_u32 s59, s68, 0
v_exp_f32 v232, v232
PW 152,184,228
v_exp_f32 v231, v231
PW 156,220,224
RQ 196,239,30720
v_mul_f32 v229, s71, v229
PW 156,188,228
v_exp_f32 v230, v230
PW 160,224,224
buffer_load_dwordx4 v237, s[44:47], s56 offen offset:256 lds
PW 160,192,228
s_waitcnt lgkmcnt(1)
v_exp_f32 v250, v250
PW 176,224,216
PW 176,192,220
v_exp_f32 v248, v248
PW 172,220,216
s_add_u32 m0, s60, 62336
v_exp_f32 v229, v229
PW 172,188,220
PW 168,216,216
s_waitcnt vmcnt(15) lgkmcnt(0)
CV 207,230,231
PW 168,184,220
s_barrier
PW 164,212,216
L_s14_%=:
buffer_load_dwordx4 v237, s[48:51], s77 offen offset:128 lds
PW 164,180,220
s_add_u32 m0, s60, 63232
RT 226,240,30720
CV 206,228,229
QC 228,212,20
RT 224,240,24576
v_mul_f32 v235, s71, v235
QK 228,208,16
v_exp_f32 v234, v234
QK 228,204,12
v_exp_f32 v235, v235
QK 228,200,8
RT 220,240,36880
CV 211,234,235
QK 228,196,4
v_exp_f32 v233, v233
QK 228,192,0
RT 218,240,30736
CV 210,232,233
QC 232,192,24
buffer_load_dwordx4 v237, s[48:51], s77 offen offset:256 lds
QK 232,196,28
QK 232,200,32
)"
R"(
CV 203,250,251
QK 232,204,36
s_add_u32 m0, s60, 75520
CV 202,248,249
v_mul_f32 v228, s71, v228
QK 232,208,40
v_exp_f32 v228, v228
QK 232,212,44
RT 222,240,43024
v_mul_f32 v229, s71, v229
QC 248,212,68
v_exp_f32 v229, v229
QK 248,208,64
v_mul_f32 v231, s71, v231
CV 212,228,229
QK 248,204,60
v_mul_f32 v230, s71, v230
QK 248,200,56
v_exp_f32 v230, v230
QK 248,196,52
v_exp_f32 v231, v231
QK 248,192,48
RT 230,240,43008
CV 213,230,231
QC 228,192,72
RT 228,240,36864
QK 228,196,76
RT 216,240,24592
v_mul_f32 v234, s71, v234
QK 228,200,80
RQ 192,238,36864
v_mul_f32 v235, s71, v235
QK 228,204,84
RQ 204,239,37888
v_mul_f32 v249, s71, v249
QK 228,208,88
RQ 200,238,37888
QK 228,212,92
v_exp_f32 v235, v235
v_smfmac_f32_16x16x64_bf16 a[232:235], a[236:239], v[196:203], v236
s_waitcnt lgkmcnt(4)
v_mul_f32 v248, s71, v248
PW 0,180,224
v_exp_f32 v249, v249
PW 0,196,228
PW 4,184,224
buffer_load_dwordx4 v237, s[48:51], s57 offen offset:256 lds
PW 4,200,228
v_exp_f32 v248, v248
PW 8,188,224
v_exp_f32 v234, v234
PW 8,204,228
RQ 196,239,36864
v_mul_f32 v230, s71, v230
PW 12,192,224
s_add_u32 s58, s70, 0
RT 226,241,30720
v_mul_f32 v251, s71, v251
PW 12,208,228
s_waitcnt lgkmcnt(2)
RT 224,241,24576
PW 28,192,216
RT 228,241,36864
v_mul_f32 v233, s71, v233
PW 28,208,220
s_add_u32 m0, s61, 10112
RT 230,241,43008
v_mul_f32 v229, s71, v229
PW 24,188,216
v_exp_f32 v251, v251
PW 24,204,220
CV 217,234,235
PW 20,184,216
buffer_load_dwordx4 v237, s[40:43], s58 offen offset:128 lds
PW 20,200,220
RQ 212,239,38912
v_mul_f32 v250, s71, v250
PW 16,180,216
PW 16,196,220
s_waitcnt lgkmcnt(1)
v_exp_f32 v233, v233
PW 32,180,224
RQ 208,238,38912
v_mul_f32 v232, s71, v232
PW 32,196,228
s_add_u32 m0, s60, 24576
v_exp_f32 v232, v232
PW 36,184,224
s_add_u32 s56, s63, 18432
PW 36,200,228
v_exp_f32 v250, v250
PW 40,188,224
RT 216,241,24592
CV 216,232,233
PW 40,204,228
RT 220,241,36880
v_mul_f32 v228, s71, v228
PW 44,192,224
buffer_load_dwordx4 v237, s[44:47], s56 offen offset:0 lds
PW 44,208,228
s_waitcnt lgkmcnt(2)
v_mul_f32 v231, s71, v231
QC 232,212,20
s_add_u32 m0, s60, 25472
v_exp_f32 v231, v231
QK 232,208,16
v_exp_f32 v230, v230
QK 232,204,12
CV 220,248,249
CV 221,250,251
QK 232,200,8
RT 228,240,37888
QK 232,196,4
v_exp_f32 v228, v228
QK 232,192,0
v_exp_f32 v229, v229
QC 248,192,24
s_waitcnt vmcnt(14)
RT 218,241,30736
CV 225,230,231
QK 248,196,28
QK 248,200,32
buffer_load_dwordx4 v237, s[44:47], s56 offen offset:128 lds
QK 248,204,36
RT 230,240,44032
v_mul_f32 v235, s71, v235
QK 248,208,40
v_exp_f32 v235, v235
QK 248,212,44
CV 224,228,229
v_mul_f32 v232, s71, v232
QC 228,212,68
v_mul_f32 v233, s71, v233
v_mul_f32 v234, s71, v234
QK 228,208,64
v_exp_f32 v234, v234
QK 228,204,60
v_exp_f32 v233, v233
QK 228,200,56
v_exp_f32 v232, v232
QK 228,196,52
s_add_u32 m0, s61, 8064
RT 222,241,43024
CV 214,232,233
QK 228,192,48
RQ 188,245,6144
CV 215,234,235
QC 232,192,72
buffer_load_dwordx4 v237, s[40:43], s58 offen offset:3200 lds
QK 232,196,76
QK 232,200,80
v_mul_f32 v249, s71, v249
QK 232,204,84
s_add_u32 m0, s60, 26368
v_mul_f32 v228, s71, v228
QK 232,208,88
v_exp_f32 v228, v228
QK 232,212,92
RT 224,240,25600
v_smfmac_f32_16x16x64_bf16 a[232:235], a[240:243], v[204:211], v236
s_waitcnt lgkmcnt(1)
RT 226,240,31744
v_mul_f32 v230, s71, v230
PW 60,192,216
RQ 184,244,6144
v_mul_f32 v251, s71, v251
PW 60,208,220
RQ 192,238,43008
PW 56,188,216
buffer_load_dwordx4 v237, s[44:47], s56 offen offset:256 lds
PW 56,204,220
v_exp_f32 v251, v251
PW 52,184,216
RQ 204,239,44032
v_mul_f32 v248, s71, v248
PW 52,200,220
RQ 212,239,45056
PW 48,180,216
RT 216,240,25616
v_mul_f32 v250, s71, v250
PW 48,196,220
s_waitcnt lgkmcnt(3)
RT 218,240,31760
PW 64,180,224
RT 222,240,44048
v_mul_f32 v235, s71, v235
PW 64,196,228
RT 220,240,37904
v_mul_f32 v231, s71, v231
PW 68,184,224
RQ 200,238,44032
v_mul_f32 v232, s71, v232
PW 68,200,228
RQ 208,238,45056
v_mul_f32 v234, s71, v234
PW 72,188,224
v_exp_f32 v234, v234
PW 72,204,228
s_add_u32 s77, s63, 12288
RQ 196,239,43008
PW 76,192,224
v_exp_f32 v230, v230
PW 76,208,228
s_waitcnt lgkmcnt(3)
v_mul_f32 v229, s71, v229
v_mul_f32 v233, s71, v233
PW 92,192,216
v_exp_f32 v231, v231
PW 92,208,220
s_add_u32 m0, s60, 73728
v_exp_f32 v232, v232
PW 88,188,216
v_exp_f32 v229, v229
PW 88,204,220
CV 222,228,229
CV 223,230,231
PW 84,184,216
s_waitcnt vmcnt(15) lgkmcnt(0)
v_exp_f32 v233, v233
PW 84,200,220
s_barrier
PW 80,180,216
L_s15_%=:
buffer_load_dwordx4 v237, s[48:51], s57 offen offset:0 lds
PW 80,196,220
v_exp_f32 v249, v249
QC 228,212,20
v_exp_f32 v235, v235
QK 228,208,16
RT 230,241,7168
QK 228,204,12
RT 226,241,44032
CV 227,234,235
QK 228,200,8
RT 224,241,37888
QK 228,196,4
v_exp_f32 v250, v250
QK 228,192,0
s_add_u32 m0, s60, 74624
RT 228,241,1024
CV 226,232,233
QC 232,192,24
RT 222,241,7184
CV 219,250,251
QK 232,196,28
buffer_load_dwordx4 v237, s[48:51], s57 offen offset:128 lds
QK 232,200,32
v_exp_f32 v248, v248
QK 232,204,36
CV 218,248,249
v_mul_f32 v229, s71, v229
QK 232,208,40
RT 218,241,44048
QK 232,212,44
RT 220,241,1040
v_smfmac_f32_16x16x64_bf16 a[232:235], a[236:239], v[212:219], v236
s_waitcnt lgkmcnt(2)
v_mul_f32 v228, s71, v228
v_mul_f32 v230, s71, v230
PW 96,196,224
RT 216,241,37904
PW 96,212,228
s_add_u32 m0, s60, 86016
v_exp_f32 v228, v228
PW 100,200,224
buffer_load_dwordx4 v237, s[48:51], s77 offen offset:0 lds
PW 100,216,228
v_mul_f32 v231, s71, v231
v_mul_f32 v232, s71, v232
PW 104,204,224
s_add_u32 m0, s61, 12032
v_exp_f32 v229, v229
PW 104,220,228
v_mul_f32 v233, s71, v233
v_mul_f32 v235, s71, v235
PW 108,208,224
s_waitcnt lgkmcnt(0)
v_exp_f32 v235, v235
PW 108,224,228
s_add_u32 s58, s70, 0
v_exp_f32 v231, v231
PW 112,208,216
v_exp_f32 v230, v230
PW 112,224,220
RT 224,240,38912
v_mfma_f32_16x16x32_bf16 a[252:255], v[204:207], a[216:219], a[252:255]
CV 180,228,229
CV 181,230,231
v_mfma_f32_16x16x32_bf16 a[252:255], v[220:223], a[220:223], a[252:255]
v_exp_f32 v233, v233
QC 228,212,68
buffer_load_dwordx4 v237, s[40:43], s58 offen offset:256 lds
QK 228,208,64
RT 228,240,2048
v_mul_f32 v234, s71, v234
QK 228,204,60
QK 228,200,56
v_exp_f32 v234, v234
QK 228,196,52
v_exp_f32 v232, v232
QK 228,192,48
CV 184,232,233
CV 185,234,235
QC 232,192,72
RT 226,240,45056
QK 232,196,76
RQ 192,238,0
QK 232,200,80
RT 230,240,8192
QK 232,204,84
v_mul_f32 v228, s71, v228
QK 232,208,88
RQ 196,239,0
v_mul_f32 v229, s71, v229
QK 232,212,92
RQ 204,239,1024
v_mul_f32 v231, s71, v231
v_mfma_f32_16x16x32_bf16 a[248:251], v[200:203], a[216:219], a[248:251]
v_exp_f32 v228, v228
v_mfma_f32_16x16x32_bf16 a[248:251], v[216:219], a[220:223], a[248:251]
s_add_u32 s56, s63, 30720
v_exp_f32 v229, v229
v_mfma_f32_16x16x32_bf16 a[244:247], v[196:199], a[216:219], a[244:247]
RQ 200,238,1024
v_mul_f32 v230, s71, v230
v_mfma_f32_16x16x32_bf16 a[244:247], v[212:215], a[220:223], a[244:247]
s_waitcnt lgkmcnt(3)
RQ 208,238,2048
CV 188,228,229
PW 116,196,224
RQ 212,239,2048
v_mul_f32 v233, s71, v233
PW 116,212,228
v_exp_f32 v231, v231
PW 120,200,224
s_add_u32 m0, s60, 36864
v_mul_f32 v232, s71, v232
v_mul_f32 v234, s71, v234
PW 120,216,228
v_exp_f32 v232, v232
PW 124,204,224
RT 216,240,38928
PW 124,220,228
v_exp_f32 v233, v233
PW 128,208,224
v_exp_f32 v230, v230
PW 128,224,228
s_waitcnt lgkmcnt(1)
RT 218,240,45072
CV 189,230,231
QC 228,212,20
RT 222,240,8208
v_mul_f32 v235, s71, v235
QK 228,208,16
L_c7_%=:
s_cbranch_execz L_c11_%=
RT 220,240,2064
QK 228,204,12
v_exp_f32 v235, v235
QK 228,200,8
CV 192,232,233
QK 228,196,4
v_exp_f32 v234, v234
QK 228,192,0
RT 228,241,2048
CV 193,234,235
QC 232,192,24
QK 232,196,28
RT 226,241,45056
QK 232,200,32
buffer_load_dwordx4 v237, s[44:47], s56 offen offset:0 lds
QK 232,204,36
v_mul_f32 v230, s71, v230
v_mul_f32 v231, s71, v231
QK 232,208,40
RT 224,241,38912
v_mul_f32 v228, s71, v228
QK 232,212,44
v_exp_f32 v230, v230
v_smfmac_f32_16x16x64_bf16 a[232:235], a[240:243], v[220:227], v236
s_waitcnt lgkmcnt(2)
RT 230,241,8192
PW 144,208,216
s_add_u32 m0, s60, 37760
v_exp_f32 v231, v231
PW 144,224,220
v_exp_f32 v228, v228
PW 140,204,216
v_mul_f32 v229, s71, v229
v_mul_f32 v234, s71, v234
PW 140,220,220
v_exp_f32 v234, v234
PW 136,200,216
v_exp_f32 v229, v229
PW 136,216,220
buffer_load_dwordx4 v237, s[44:47], s56 offen offset:128 lds
PW 132,196,216
RT 218,241,45072
PW 132,212,220
s_waitcnt lgkmcnt(1)
)"
R"(
RT 220,241,2064
v_mul_f32 v232, s71, v232
PW 148,196,224
v_mul_f32 v233, s71, v233
PW 148,212,228
v_mul_f32 v235, s71, v235
PW 152,200,224
v_exp_f32 v232, v232
PW 152,216,228
CV 182,228,229
CV 183,230,231
QC 228,212,68
s_add_u32 m0, s61, 9984
v_exp_f32 v233, v233
QK 228,208,64
v_exp_f32 v235, v235
QK 228,204,60
CV 187,234,235
QK 228,200,56
buffer_load_dwordx4 v237, s[40:43], s58 offen offset:3328 lds
QK 228,196,52
RT 222,241,8208
QK 228,192,48
RT 216,241,38928
CV 186,232,233
QC 232,192,72
RQ 192,238,6144
QK 232,196,76
RQ 196,239,6144
QK 232,200,80
RQ 200,238,7168
QK 232,204,84
s_add_u32 m0, s60, 38656
QK 232,208,88
RQ 204,239,7168
v_mul_f32 v228, s71, v228
QK 232,212,92
RQ 208,238,8192
v_smfmac_f32_16x16x64_bf16 a[232:235], a[236:239], v[180:187], v236
v_mul_f32 v230, s71, v230
v_mul_f32 v231, s71, v231
PW 156,204,224
buffer_load_dwordx4 v237, s[44:47], s56 offen offset:256 lds
PW 156,220,228
s_add_u32 m0, s60, 86912
RQ 212,239,8192
v_mul_f32 v229, s71, v229
PW 160,208,224
v_mul_f32 v233, s71, v233
v_mul_f32 v235, s71, v235
PW 160,224,228
s_waitcnt lgkmcnt(2)
v_exp_f32 v230, v230
PW 176,208,216
s_add_u32 s57, s63, 24576
v_exp_f32 v228, v228
PW 176,224,220
v_exp_f32 v231, v231
PW 172,204,216
CV 191,230,231
v_mul_f32 v234, s71, v234
PW 172,220,220
v_exp_f32 v229, v229
PW 168,200,216
s_waitcnt vmcnt(15) lgkmcnt(0)
v_exp_f32 v235, v235
PW 168,216,220
s_barrier
PW 164,196,216
L_s16_%=:
buffer_load_dwordx4 v237, s[48:51], s77 offen offset:128 lds
PW 164,212,220
RT 230,240,18432
CV 190,228,229
QC 228,212,116
v_exp_f32 v233, v233
QK 228,208,112
RT 226,240,6144
v_mul_f32 v232, s71, v232
QK 228,204,108
s_add_u32 m0, s60, 87808
v_exp_f32 v234, v234
QK 228,200,104
v_exp_f32 v232, v232
QK 228,196,100
RT 228,240,12288
CV 195,234,235
QK 228,192,96
RT 224,240,0
CV 194,232,233
QC 232,192,120
buffer_load_dwordx4 v237, s[48:51], s77 offen offset:256 lds
QK 232,196,124
RT 220,240,12304
QK 232,200,128
s_add_u32 m0, s60, 50944
RT 218,240,6160
QK 232,204,132
RT 216,240,16
v_mul_f32 v231, s71, v231
QK 232,208,136
v_mul_f32 v229, s71, v229
v_mul_f32 v230, s71, v230
QK 232,212,140
v_exp_f32 v229, v229
v_smfmac_f32_16x16x64_bf16 a[232:235], a[240:243], v[188:195], v236
s_waitcnt lgkmcnt(3)
RT 222,240,18448
v_mul_f32 v228, s71, v228
PW 0,212,224
v_exp_f32 v228, v228
PW 0,180,228
buffer_load_dwordx4 v237, s[48:51], s57 offen offset:256 lds
PW 4,216,224
v_exp_f32 v230, v230
PW 4,184,228
v_accvgpr_read_b32 v252, a232
v_mul_f32 v233, s71, v233
PW 8,220,224
v_exp_f32 v231, v231
PW 8,188,228
v_accvgpr_read_b32 v248, a233
v_accvgpr_write_b32 a232, 0
PW 12,224,224
RT 224,241,0
CV 196,228,229
PW 12,192,228
s_waitcnt lgkmcnt(1)
v_exp_f32 v233, v233
PW 28,224,216
CV 197,230,231
v_mul_f32 v235, s71, v235
PW 28,192,220
s_mov_b32 m0, s80
v_mul_f32 v232, s71, v232
PW 24,220,216
v_mul_f32 v234, s71, v234
PW 24,188,220
v_exp_f32 v234, v234
QC 228,212,164
RT 230,241,18432
QK 228,208,160
v_exp_f32 v232, v232
QK 228,204,156
s_add_u32 s77, s63, 36864
v_exp_f32 v235, v235
QK 228,200,152
v_add_f32 v252, v252, v248
QK 228,196,148
s_add_u32 s56, s63, 43008
v_rcp_f32 v252, v252
QK 228,192,144
CV 200,232,233
CV 201,234,235
QC 232,192,168
RT 228,241,12288
QK 232,196,172
ds_write_addtid_b32 v252 offset:65280
QK 232,200,176
RT 226,241,6144
QK 232,204,180
RQ 196,239,12288
v_mul_f32 v229, s71, v229
QK 232,208,184
v_exp_f32 v229, v229
QK 232,212,188
ds_read_b128 v[248:251], v254 offset:0
v_mul_f32 v231, s71, v231
PW 20,216,216
RQ 212,239,14336
PW 20,184,220
v_exp_f32 v231, v231
PW 16,212,216
s_add_u32 s58, s70, 6144
RQ 204,239,13312
v_cmp_gt_u32 s[78:79], s74, v252
PW 16,180,220
s_waitcnt lgkmcnt(3)
RQ 200,238,13312
v_mul_f32 v230, s71, v230
PW 32,212,224
s_add_u32 m0, s61, 14336
RQ 208,238,14336
v_mul_f32 v232, s71, v232
PW 32,180,228
RQ 192,238,12288
v_mul_f32 v228, s71, v228
PW 36,216,224
v_mul_f32 v234, s71, v234
v_mul_f32 v235, s71, v235
PW 36,184,228
v_exp_f32 v235, v235
PW 40,220,224
v_exp_f32 v230, v230
PW 40,188,228
v_exp_f32 v228, v228
PW 44,224,224
RT 220,241,12304
CV 204,228,229
PW 44,192,228
s_waitcnt lgkmcnt(1)
v_cmp_lt_u32 vcc, s75, v252
CV 205,230,231
QC 228,212,116
RT 216,241,16
QK 228,208,112
v_exp_f32 v234, v234
QK 228,204,108
v_exp_f32 v232, v232
QK 228,200,104
buffer_load_dwordx4 v237, s[40:43], s58 offen offset:0 lds
RT 218,241,6160
v_mul_f32 v233, s71, v233
v_pk_mul_f32 v[0:1], v[0:1], v[248:249]
v_pk_mul_f32 v[2:3], v[2:3], v[250:251]
v_pk_mul_f32 v[16:17], v[16:17], v[248:249]
v_pk_mul_f32 v[18:19], v[18:19], v[250:251]
v_pk_mul_f32 v[32:33], v[32:33], v[248:249]
v_pk_mul_f32 v[34:35], v[34:35], v[250:251]
CV 209,234,235
QK 228,196,100
v_exp_f32 v233, v233
QK 228,192,96
RT 222,241,18448
CV 208,232,233
QC 232,192,120
RT 224,240,1024
v_accvgpr_write_b32 a233, 0
QK 232,196,124
CV 0,0,16
CV 16,2,18
QK 232,200,128
RT 228,240,13312
CV 18,3,19
QK 232,204,132
v_mul_f32 v230, s71, v230
v_mul_f32 v231, s71, v231
QK 232,208,136
s_add_u32 m0, s60, 0
RT 230,240,19456
QK 232,212,140
s_waitcnt lgkmcnt(3)
buffer_load_dwordx4 v237, s[44:47], s56 offen offset:0 lds
PW 60,224,216
RT 226,240,7168
v_mul_f32 v229, s71, v229
PW 60,192,220
v_exp_f32 v229, v229
PW 56,220,216
v_mul_f32 v228, s71, v228
CV 2,1,17
PW 56,188,220
v_exp_f32 v230, v230
PW 52,216,216
s_add_u32 m0, s60, 896
v_exp_f32 v231, v231
PW 52,184,220
CV 199,230,231
v_mul_f32 v233, s71, v233
PW 48,212,216
RT 216,240,1040
v_mul_f32 v235, s71, v235
PW 48,180,220
s_waitcnt lgkmcnt(1)
buffer_load_dwordx4 v237, s[44:47], s56 offen offset:128 lds
PW 64,212,224
RT 218,240,7184
v_mul_f32 v232, s71, v232
PW 64,180,228
v_exp_f32 v228, v228
PW 68,216,224
RT 222,240,19472
CV 198,228,229
PW 68,184,228
s_add_u32 m0, s61, 12288
v_mul_f32 v234, s71, v234
v_exp_f32 v232, v232
v_exp_f32 v233, v233
v_pk_mul_f32 v[48:49], v[48:49], v[248:249]
v_pk_mul_f32 v[50:51], v[50:51], v[250:251]
CV 3,33,49
QC 228,212,164
ds_read_b128 v[248:251], v254 offset:64
CV 202,232,233
QK 228,208,160
v_exp_f32 v235, v235
QK 228,204,156
v_exp_f32 v234, v234
QK 228,200,152
CV 17,34,50
CV 203,234,235
QK 228,196,148
buffer_load_dwordx4 v237, s[40:43], s58 offen offset:3072 lds
QK 228,192,144
RT 220,240,13328
CV 19,35,51
QC 232,192,168
s_waitcnt lgkmcnt(1)
ds_write_b128 v246, v[16:19] offset:1280
v_pk_mul_f32 v[4:5], v[4:5], v[248:249]
v_pk_mul_f32 v[6:7], v[6:7], v[250:251]
v_pk_mul_f32 v[20:21], v[20:21], v[248:249]
v_pk_mul_f32 v[22:23], v[22:23], v[250:251]
v_pk_mul_f32 v[36:37], v[36:37], v[248:249]
v_pk_mul_f32 v[38:39], v[38:39], v[250:251]
v_pk_mul_f32 v[52:53], v[52:53], v[248:249]
v_pk_mul_f32 v[54:55], v[54:55], v[250:251]
CV 1,32,48
QK 232,196,172
ds_read_b128 v[248:251], v254 offset:128
CV 4,4,20
QK 232,200,176
RQ 196,239,18432
CV 20,6,22
QK 232,204,180
RQ 204,239,19456
CV 6,5,21
QK 232,208,184
RQ 200,238,19456
v_mul_f32 v228, s71, v228
QK 232,212,188
RQ 208,238,20480
CV 21,38,54
PW 72,220,224
RQ 212,239,20480
v_mul_f32 v230, s71, v230
PW 72,188,228
RQ 192,238,18432
v_mul_f32 v231, s71, v231
PW 76,224,224
s_add_u32 m0, s60, 1792
v_exp_f32 v230, v230
PW 76,192,228
s_waitcnt lgkmcnt(4)
ds_write_b128 v246, v[0:3] offset:0
v_pk_mul_f32 v[8:9], v[8:9], v[248:249]
v_pk_mul_f32 v[10:11], v[10:11], v[250:251]
v_pk_mul_f32 v[24:25], v[24:25], v[248:249]
v_pk_mul_f32 v[26:27], v[26:27], v[250:251]
v_pk_mul_f32 v[56:57], v[56:57], v[248:249]
v_pk_mul_f32 v[58:59], v[58:59], v[250:251]
v_exp_f32 v228, v228
PW 92,224,216
RT 6,247,1600
v_mul_f32 v229, s71, v229
v_exp_f32 v229, v229
CV 8,8,24
v_pk_mul_f32 v[40:41], v[40:41], v[248:249]
v_pk_mul_f32 v[42:43], v[42:43], v[250:251]
v_mul_f32 v232, s71, v232
PW 92,192,220
ds_read_b128 v[248:251], v254 offset:192
CV 22,7,23
PW 88,220,216
buffer_load_dwordx4 v237, s[44:47], s56 offen offset:256 lds
v_mul_f32 v234, s71, v234
PW 88,188,220
RT 4,247,320
CV 5,36,52
v_mul_f32 v235, s71, v235
PW 84,216,216
s_waitcnt vmcnt(14) lgkmcnt(4)
RT 0,247,0
v_exp_f32 v234, v234
PW 84,184,220
s_barrier
PW 80,212,216
L_s17_%=:
v_exp_f32 v231, v231
CV 206,228,229
PW 80,180,220
s_add_u32 m0, s60, 50048
RT 226,241,19456
CV 207,230,231
CV 24,10,26
QC 228,212,116
RT 230,241,19472
v_mul_f32 v233, s71, v233
QK 228,208,112
RT 2,247,1280
v_exp_f32 v235, v235
QK 228,204,108
s_waitcnt lgkmcnt(3)
buffer_load_dwordx4 v237, s[48:51], s57 offen offset:128 lds
RT 224,241,13312
CV 7,37,53
CV 26,11,27
v_pk_mul_f32 v[14:15], v[14:15], v[250:251]
v_pk_mul_f32 v[28:29], v[28:29], v[248:249]
v_pk_mul_f32 v[44:45], v[44:45], v[248:249]
v_pk_mul_f32 v[46:47], v[46:47], v[250:251]
v_pk_mul_f32 v[60:61], v[60:61], v[248:249]
v_pk_mul_f32 v[62:63], v[62:63], v[250:251]
v_exp_f32 v232, v232
QK 228,200,104
RT 228,241,13328
v_exp_f32 v233, v233
QK 228,196,100
CV 210,232,233
CV 211,234,235
)"
R"(
QK 228,192,96
s_add_u32 m0, s60, 49152
ds_write_b128 v246, v[4:7] offset:0
v_pk_mul_f32 v[12:13], v[12:13], v[248:249]
v_pk_mul_f32 v[30:31], v[30:31], v[250:251]
CV 23,39,55
QC 232,192,120
CV 10,9,25
CV 11,41,57
QK 232,196,124
RT 12,247,320
CV 12,12,28
QK 232,200,128
buffer_store_dwordx2 a[0:1], v242, s[52:55], s59 offen offset:0 nt
ds_read_b128 v[248:251], v254 offset:0
QK 232,204,132
RT 222,240,20496
CV 25,42,58
v_mul_f32 v229, s71, v229
QK 232,208,136
ds_write_b128 v246, v[20:23] offset:1280
QK 232,212,140
CV 27,43,59
v_mul_f32 v231, s71, v231
v_smfmac_f32_16x16x64_bf16 a[232:235], a[236:239], v[196:203], v236
s_waitcnt lgkmcnt(3)
buffer_load_dwordx4 v237, s[48:51], s57 offen offset:0 lds
RT 216,240,14336
PW 96,180,224
RT 218,240,20480
v_mul_f32 v228, s71, v228
v_exp_f32 v231, v231
PW 100,184,224
RT 10,247,1280
v_mul_f32 v230, s71, v230
v_mfma_f32_16x16x32_bf16 a[244:247], v[180:183], a[228:231], a[244:247]
s_waitcnt lgkmcnt(3)
RT 220,240,14352
CV 9,40,56
v_mfma_f32_16x16x32_bf16 a[248:251], v[184:187], a[228:231], a[248:251]
s_add_u32 m0, s60, 61440
RT 8,247,0
ds_write_b128 v246, v[8:11] offset:0
v_exp_f32 v228, v228
v_exp_f32 v230, v230
v_pk_mul_f32 v[64:65], v[64:65], v[248:249]
v_pk_mul_f32 v[66:67], v[66:67], v[250:251]
v_pk_mul_f32 v[80:81], v[80:81], v[248:249]
v_pk_mul_f32 v[82:83], v[82:83], v[250:251]
CV 64,64,80
CV 80,66,82
PW 104,188,224
s_add_u32 s58, s70, 6144
CV 28,14,30
v_mul_f32 v235, s71, v235
PW 108,192,224
buffer_store_dwordx2 a[2:3], v242, s[52:55], s59 offen offset:768 nt
RT 14,247,1600
v_mul_f32 v233, s71, v233
CV 66,65,81
v_pk_mul_f32 v[98:99], v[98:99], v[250:251]
CV 82,67,83
v_mfma_f32_16x16x32_bf16 a[252:255], v[188:191], a[228:231], a[252:255]
v_exp_f32 v229, v229
v_accvgpr_read_b32 v83, a247
PW 112,192,228
s_waitcnt lgkmcnt(1)
v_exp_f32 v235, v235
PW 128,192,216
RT 16,247,0
ds_write_b128 v246, v[24:27] offset:1280
v_pk_mul_f32 v[96:97], v[96:97], v[248:249]
CV 213,230,231
PW 124,188,216
CV 212,228,229
v_mul_f32 v234, s71, v234
v_accvgpr_read_b32 v81, a246
v_exp_f32 v234, v234
PW 144,192,220
CV 30,15,31
v_exp_f32 v233, v233
v_accvgpr_read_b32 v65, a244
v_mul_f32 v83, v83, v251
PW 140,188,220
buffer_load_dwordx4 v237, s[48:51], s77 offen offset:0 lds
RT 224,241,14336
CV 14,13,29
CV 15,45,61
QC 228,212,164
RT 18,247,1280
v_mul_f32 v232, s71, v232
v_mul_f32 v65, v65, v248
QK 228,208,160
RT 22,247,1600
v_accvgpr_read_b32 v67, a245
QK 228,204,156
RT 226,241,20480
v_mul_f32 v81, v81, v250
QK 228,200,152
s_add_u32 m0, s61, 3968
RT 230,241,20496
v_exp_f32 v232, v232
QK 228,196,148
CV 13,44,60
CV 217,234,235
QK 228,192,144
CV 31,47,63
v_mul_f32 v67, v67, v249
CV 216,232,233
QC 232,192,168
ds_read_b128 v[248:251], v254 offset:64
CV 81,98,81
QK 232,196,172
RT 228,241,14352
CV 29,46,62
QK 232,200,176
buffer_load_dwordx4 v237, s[40:43], s58 offen offset:128 lds
QK 232,204,180
CV 83,99,83
v_mul_f32 v231, s71, v231
QK 232,208,184
RQ 196,239,24576
v_mul_f32 v230, s71, v230
QK 232,212,188
RQ 204,239,25600
v_mul_f32 v228, s71, v228
v_exp_f32 v230, v230
PW 120,184,216
RQ 208,238,26624
v_exp_f32 v228, v228
PW 116,180,216
s_waitcnt lgkmcnt(3)
RT 20,247,320
ds_write_b128 v246, v[28:31] offset:1280
v_pk_mul_f32 v[68:69], v[68:69], v[248:249]
v_pk_mul_f32 v[70:71], v[70:71], v[250:251]
v_pk_mul_f32 v[84:85], v[84:85], v[248:249]
v_pk_mul_f32 v[86:87], v[86:87], v[250:251]
CV 68,68,84
CV 84,70,86
CV 70,69,85
CV 86,71,87
PW 136,184,220
L_c8_%=:
s_cbranch_execz L_c12_%=
RQ 192,238,24576
ds_write_b128 v246, v[12:15] offset:0
v_accvgpr_read_b32 v71, a249
v_accvgpr_read_b32 v85, a250
v_accvgpr_read_b32 v87, a251
v_mul_f32 v85, v85, v250
v_mul_f32 v87, v87, v251
v_pk_mul_f32 v[100:101], v[100:101], v[248:249]
v_pk_mul_f32 v[102:103], v[102:103], v[250:251]
v_exp_f32 v231, v231
PW 132,180,220
s_add_u32 s56, s63, 55296
RQ 212,239,26624
v_accvgpr_read_b32 v69, a248
v_mul_f32 v69, v69, v248
v_mul_f32 v71, v71, v249
v_mul_f32 v235, s71, v235
PW 148,180,224
ds_read_b128 v[248:251], v254 offset:128
v_mul_f32 v233, s71, v233
PW 152,184,224
s_add_u32 m0, s60, 12288
RQ 200,238,25600
v_mul_f32 v234, s71, v234
PW 164,180,228
RT 24,247,0
v_mul_f32 v229, s71, v229
v_exp_f32 v234, v234
PW 168,184,228
RT 218,240,30720
v_exp_f32 v229, v229
CV 221,230,231
PW 156,188,224
RT 222,240,30736
CV 65,96,65
CV 71,101,71
CV 220,228,229
v_mul_f32 v232, s71, v232
PW 160,192,224
RT 26,247,1280
v_exp_f32 v232, v232
v_exp_f32 v233, v233
PW 172,188,228
RT 28,247,320
CV 67,97,67
PW 176,192,228
s_waitcnt lgkmcnt(3)
RT 30,247,1600
ds_write_b128 v246, v[80:83] offset:1280
CV 85,102,85
v_pk_mul_f32 v[72:73], v[72:73], v[248:249]
v_pk_mul_f32 v[74:75], v[74:75], v[250:251]
v_pk_mul_f32 v[88:89], v[88:89], v[248:249]
v_pk_mul_f32 v[90:91], v[90:91], v[250:251]
CV 72,72,88
CV 88,74,90
CV 90,75,91
v_pk_mul_f32 v[106:107], v[106:107], v[250:251]
v_accvgpr_read_b32 v75, a253
v_accvgpr_read_b32 v91, a255
QC 228,212,116
RT 216,240,24576
ds_write_b128 v246, v[64:67] offset:0
v_pk_mul_f32 v[104:105], v[104:105], v[248:249]
v_mul_f32 v91, v91, v251
QK 228,208,112
CV 74,73,89
v_accvgpr_read_b32 v73, a252
v_accvgpr_read_b32 v89, a254
QK 228,204,108
RT 220,240,24592
v_mul_f32 v73, v73, v248
v_mul_f32 v75, v75, v249
v_mul_f32 v89, v89, v250
QK 228,200,104
ds_read_b128 v[248:251], v254 offset:192
v_exp_f32 v235, v235
QK 228,196,100
CV 91,107,91
CV 224,232,233
QK 228,192,96
RT 32,247,0
CV 225,234,235
QC 232,192,120
RT 34,247,1280
CV 87,103,87
QK 232,196,124
buffer_load_dwordx4 v237, s[44:47], s56 offen offset:0 lds
QK 232,200,128
RT 226,241,30720
CV 69,100,69
QK 232,204,132
RT 228,241,24592
v_mul_f32 v231, s71, v231
QK 232,208,136
s_waitcnt lgkmcnt(4)
RT 38,247,1600
ds_write_b128 v246, v[84:87] offset:1280
v_pk_mul_f32 v[76:77], v[76:77], v[248:249]
v_pk_mul_f32 v[78:79], v[78:79], v[250:251]
v_pk_mul_f32 v[92:93], v[92:93], v[248:249]
v_pk_mul_f32 v[94:95], v[94:95], v[250:251]
v_pk_mul_f32 v[108:109], v[108:109], v[248:249]
v_pk_mul_f32 v[110:111], v[110:111], v[250:251]
v_pk_mul_f32 v[112:113], v[112:113], v[248:249]
v_pk_mul_f32 v[114:115], v[114:115], v[250:251]
v_mul_f32 v228, s71, v228
QK 232,212,140
ds_read_b128 v[248:251], v254 offset:0
v_mul_f32 v229, s71, v229
v_smfmac_f32_16x16x64_bf16 a[232:235], a[240:243], v[204:211], v236
RT 230,241,30736
v_exp_f32 v229, v229
PX 12,208,216
s_add_u32 m0, s60, 13184
RT 224,241,24576
CV 75,105,75
PX 8,204,216
buffer_store_dwordx2 a[4:5], v242, s[52:55], s59 offen offset:1536 nt
RT 36,247,320
CV 76,76,92
PX 28,208,220
RT 46,247,1600
CV 89,106,89
PX 24,204,220
ds_write_b128 v246, v[68:71] offset:0
v_exp_f32 v228, v228
PX 4,200,216
CV 73,104,73
v_mul_f32 v230, s71, v230
PX 0,196,216
s_waitcnt lgkmcnt(3)
RT 42,247,1280
ds_write_b128 v246, v[88:91] offset:1280
v_pk_mul_f32 v[132:133], v[132:133], v[248:249]
v_pk_mul_f32 v[134:135], v[134:135], v[250:251]
v_pk_mul_f32 v[148:149], v[148:149], v[248:249]
v_pk_mul_f32 v[150:151], v[150:151], v[250:251]
v_pk_mul_f32 v[166:167], v[166:167], v[250:251]
CV 92,78,94
PX 20,200,220
buffer_load_dwordx4 v237, s[44:47], s56 offen offset:128 lds
v_exp_f32 v231, v231
v_mul_f32 v232, s71, v232
v_pk_mul_f32 v[116:117], v[116:117], v[248:249]
v_pk_mul_f32 v[118:119], v[118:119], v[250:251]
v_pk_mul_f32 v[164:165], v[164:165], v[248:249]
v_mul_f32 v235, s71, v235
PX 16,196,220
s_add_u32 m0, s61, 1920
ds_read_b128 v[248:251], v254 offset:64
v_mul_f32 v234, s71, v234
PX 32,196,224
RT 220,240,25616
v_mul_f32 v233, s71, v233
PX 36,200,224
v_exp_f32 v230, v230
CV 214,228,229
PX 48,196,228
RT 216,240,25600
CV 215,230,231
PX 52,200,228
CV 78,77,93
v_exp_f32 v233, v233
QC 228,212,164
RT 40,247,0
CV 94,79,95
QK 228,208,160
s_waitcnt lgkmcnt(2)
RT 54,247,1600
v_exp_f32 v234, v234
QK 228,204,156
buffer_load_dwordx4 v237, s[40:43], s58 offen offset:3200 lds
RT 44,247,320
v_exp_f32 v232, v232
v_pk_mul_f32 v[122:123], v[122:123], v[250:251]
v_pk_mul_f32 v[136:137], v[136:137], v[248:249]
v_pk_mul_f32 v[138:139], v[138:139], v[250:251]
v_pk_mul_f32 v[168:169], v[168:169], v[248:249]
v_pk_mul_f32 v[170:171], v[170:171], v[250:251]
v_exp_f32 v235, v235
QK 228,200,152
CV 95,111,115
CV 219,234,235
QK 228,196,148
RT 218,240,31744
ds_write_b128 v246, v[72:75] offset:0
v_pk_mul_f32 v[120:121], v[120:121], v[248:249]
v_pk_mul_f32 v[152:153], v[152:153], v[248:249]
v_pk_mul_f32 v[154:155], v[154:155], v[250:251]
CV 218,232,233
QK 228,192,144
buffer_store_dwordx2 a[6:7], v242, s[52:55], s59 offen offset:2304 nt
RT 222,240,31760
QC 232,192,168
ds_read_b128 v[248:251], v254 offset:128
CV 120,120,136
QK 232,196,172
RQ 196,239,30720
CV 77,108,112
QK 232,200,176
s_or_b64 s[78:79], s[78:79], vcc
s_cselect_b32 s73, 1, 0
RT 52,247,320
CV 116,116,132
QK 232,204,180
RQ 200,238,31744
v_mul_f32 v231, s71, v231
QK 232,208,184
s_add_u32 m0, s60, 14080
RQ 192,238,30720
v_mul_f32 v230, s71, v230
QK 232,212,188
buffer_load_dwordx4 v237, s[44:47], s56 offen offset:256 lds
RQ 208,238,32768
PX 40,204,224
RQ 212,239,32768
CV 132,118,134
PX 44,208,224
RQ 204,239,31744
CV 79,109,113
v_mul_f32 v229, s71, v229
PX 56,204,228
s_waitcnt lgkmcnt(4)
RT 48,247,0
ds_write_b128 v246, v[76:79] offset:0
v_pk_mul_f32 v[172:173], v[172:173], v[248:249]
v_pk_mul_f32 v[174:175], v[174:175], v[250:251]
v_mul_f32 v228, s71, v228
PX 60,208,228
s_add_u32 s59, s68, 6144
CV 93,110,114
v_exp_f32 v228, v228
v_exp_f32 v229, v229
v_pk_mul_f32 v[124:125], v[124:125], v[248:249]
v_pk_mul_f32 v[142:143], v[142:143], v[250:251]
v_pk_mul_f32 v[156:157], v[156:157], v[248:249]
v_exp_f32 v230, v230
PX 76,208,216
s_add_u32 m0, s60, 62336
v_mul_f32 v234, s71, v234
v_mul_f32 v235, s71, v235
PX 72,204,216
RT 50,247,1280
ds_write_b128 v246, v[92:95] offset:1280
v_pk_mul_f32 v[126:127], v[126:127], v[250:251]
v_pk_mul_f32 v[140:141], v[140:141], v[248:249]
v_pk_mul_f32 v[158:159], v[158:159], v[250:251]
v_mul_f32 v232, s71, v232
PX 92,208,220
CV 134,119,135
v_exp_f32 v234, v234
PX 88,204,220
v_exp_f32 v231, v231
PX 68,200,216
s_waitcnt vmcnt(19) lgkmcnt(4)
)"
R"(
ds_read_b128 v[248:251], v254 offset:192
CV 223,230,231
PX 64,196,216
s_barrier
RT 228,240,36880
CV 222,228,229
PX 84,200,220
L_s18_%=:
v_mul_f32 v233, s71, v233
v_exp_f32 v233, v233
PX 80,196,220
RT 58,247,1280
CV 118,117,133
QC 228,212,116
v_exp_f32 v235, v235
QK 228,208,112
s_add_u32 s58, s70, 6144
RT 224,240,36864
CV 133,150,166
CV 227,234,235
QK 228,204,108
RT 226,240,43008
CV 135,151,167
QK 228,200,104
v_exp_f32 v232, v232
QK 228,196,100
s_add_u32 s57, s63, 49152
RT 216,241,36864
CV 136,122,138
QK 228,192,96
RT 62,247,1600
CV 226,232,233
QC 232,192,120
s_waitcnt vmcnt(37) lgkmcnt(4)
RQ 0,244,8192
CV 122,121,137
QK 232,196,124
RT 230,240,43024
CV 117,148,164
QK 232,200,128
buffer_load_dwordx4 v237, s[48:51], s77 offen offset:128 lds
RT 218,241,43008
QK 232,204,132
RT 222,241,43024
CV 119,149,165
QK 232,208,136
s_or_b32 s72, s72, s73
RT 60,247,320
v_mul_f32 v230, s71, v230
QK 232,212,140
s_add_u32 m0, s60, 63232
ds_write_b128 v246, v[132:135] offset:1280
v_pk_mul_f32 v[128:129], v[128:129], v[248:249]
v_pk_mul_f32 v[130:131], v[130:131], v[250:251]
v_pk_mul_f32 v[146:147], v[146:147], v[250:251]
v_mul_f32 v231, s71, v231
v_smfmac_f32_16x16x64_bf16 a[232:235], a[236:239], v[212:219], v236
s_waitcnt lgkmcnt(3)
CV 138,123,139
CV 137,154,170
PW 0,212,224
v_exp_f32 v231, v231
PW 4,216,224
RT 220,241,36880
v_mul_f32 v229, s71, v229
PW 16,212,228
v_mul_f32 v228, s71, v228
v_mul_f32 v232, s71, v232
PW 20,216,228
buffer_load_dwordx4 v237, s[48:51], s77 offen offset:256 lds
PW 8,220,224
RT 56,247,0
v_mul_f32 v235, s71, v235
PW 12,224,224
s_add_u32 m0, s60, 73728
v_exp_f32 v229, v229
PW 24,220,228
RT 70,247,1600
v_exp_f32 v228, v228
PW 28,224,228
v_exp_f32 v230, v230
PW 44,224,216
ds_write_b128 v246, v[116:119] offset:0
v_pk_mul_f32 v[144:145], v[144:145], v[248:249]
v_pk_mul_f32 v[160:161], v[160:161], v[248:249]
v_pk_mul_f32 v[162:163], v[162:163], v[250:251]
v_pk_mul_f32 v[176:177], v[176:177], v[248:249]
v_pk_mul_f32 v[178:179], v[178:179], v[250:251]
v_mul_f32 v234, s71, v234
PW 40,220,216
s_waitcnt lgkmcnt(1)
CV 181,230,231
v_mul_f32 v233, s71, v233
v_exp_f32 v233, v233
PW 60,224,220
buffer_store_dwordx2 a[8:9], v242, s[52:55], s59 offen offset:0 nt
RT 230,240,44048
PW 56,220,220
RQ 4,245,8192
CV 180,228,229
v_exp_f32 v235, v235
QC 228,212,164
buffer_load_dwordx4 v237, s[48:51], s57 offen offset:0 lds
QK 228,208,160
v_exp_f32 v232, v232
QK 228,204,156
v_exp_f32 v234, v234
QK 228,200,152
s_add_u32 m0, s61, 5888
RT 224,240,37888
CV 185,234,235
QK 228,196,148
RT 68,247,320
CV 184,232,233
QK 228,192,144
buffer_store_dwordx2 a[10:11], v242, s[52:55], s59 offen offset:768 nt
RT 228,240,37904
QC 232,192,168
RT 66,247,1280
CV 139,155,171
QK 232,196,172
RT 64,247,0
CV 124,124,140
QK 232,200,176
RT 226,240,44032
CV 123,153,169
QK 232,204,180
RQ 192,238,36864
v_mul_f32 v228, s71, v228
v_exp_f32 v228, v228
QK 232,208,184
v_mul_f32 v229, s71, v229
v_mul_f32 v231, s71, v231
QK 232,212,188
RQ 208,238,38912
v_mul_f32 v230, s71, v230
PW 36,216,216
RQ 200,238,37888
v_exp_f32 v231, v231
PW 32,212,216
s_add_u32 s56, s63, 67584
RQ 212,239,38912
CV 121,152,168
CV 140,126,142
PW 52,216,220
buffer_load_dwordx4 v237, s[40:43], s58 offen offset:256 lds
PW 48,212,220
s_waitcnt lgkmcnt(3)
v_exp_f32 v230, v230
v_mul_f32 v232, s71, v232
PW 64,212,224
RQ 204,239,37888
v_mul_f32 v234, s71, v234
PW 68,216,224
RT 220,241,25616
v_mul_f32 v235, s71, v235
PW 80,212,228
RQ 196,239,36864
v_exp_f32 v229, v229
PW 84,216,228
s_add_u32 m0, s60, 24576
CV 126,125,141
CV 189,230,231
PW 72,220,224
RT 222,241,31760
v_mul_f32 v233, s71, v233
PW 76,224,224
ds_write_b128 v246, v[120:123] offset:0
PW 88,220,228
s_waitcnt lgkmcnt(3)
CV 188,228,229
v_exp_f32 v232, v232
PW 92,224,228
buffer_load_dwordx4 v237, s[44:47], s56 offen offset:0 lds
RT 72,247,0
QC 228,212,116
RT 224,240,26624
v_exp_f32 v235, v235
QK 228,208,112
s_waitcnt lgkmcnt(3)
ds_write_b128 v246, v[136:139] offset:1280
QK 228,204,108
s_add_u32 s77, s63, 61440
v_exp_f32 v234, v234
QK 228,200,104
s_add_u32 m0, s60, 25472
CV 142,127,143
v_exp_f32 v233, v233
QK 228,196,100
RT 216,241,25600
CV 192,232,233
CV 193,234,235
QK 228,192,96
RT 218,241,31744
CV 141,158,174
QC 232,192,120
RT 230,240,32784
CV 128,128,144
QK 232,196,124
buffer_load_dwordx4 v237, s[44:47], s56 offen offset:128 lds
RT 78,247,1600
QK 232,200,128
RT 228,240,26640
CV 143,159,175
QK 232,204,132
CV 127,157,173
CV 144,130,146
v_mul_f32 v228, s71, v228
v_mul_f32 v231, s71, v231
QK 232,208,136
RT 74,247,1280
v_mul_f32 v230, s71, v230
QK 232,212,140
s_add_u32 m0, s61, 3840
RT 226,240,32768
CV 146,131,147
v_smfmac_f32_16x16x64_bf16 a[232:235], a[240:243], v[220:227], v236
s_waitcnt lgkmcnt(4)
v_exp_f32 v231, v231
PX 108,208,216
ds_write_b128 v246, v[140:143] offset:1280
PX 104,204,216
v_exp_f32 v230, v230
PX 112,208,220
buffer_store_dwordx2 a[12:13], v242, s[52:55], s59 offen offset:1536 nt
RT 86,247,1600
v_mfma_f32_16x16x32_bf16 a[252:255], v[204:207], a[220:223], 0
RT 82,247,1280
v_mul_f32 v234, s71, v234
PX 100,200,216
v_mul_f32 v232, s71, v232
v_exp_f32 v234, v234
PX 96,196,216
RT 76,247,320
v_mul_f32 v233, s71, v233
v_mfma_f32_16x16x32_bf16 a[248:251], v[200:203], a[220:223], 0
CV 125,156,172
CV 183,230,231
v_exp_f32 v233, v233
v_mfma_f32_16x16x32_bf16 a[244:247], v[196:199], a[220:223], 0
s_waitcnt lgkmcnt(2)
RT 222,241,32784
v_mul_f32 v229, s71, v229
PX 116,196,224
ds_write_b128 v246, v[124:127] offset:0
PX 120,200,224
v_exp_f32 v228, v228
v_mul_f32 v235, s71, v235
PX 132,196,228
buffer_store_dwordx2 a[14:15], v242, s[52:55], s59 offen offset:2304 nt
v_exp_f32 v229, v229
PX 136,200,228
CV 130,129,145
CV 182,228,229
QC 228,212,164
RT 218,241,32768
CV 131,161,177
QK 228,208,160
RT 216,241,26624
v_exp_f32 v232, v232
QK 228,204,156
RT 80,247,0
CV 186,232,233
QK 228,200,152
RT 220,241,26640
CV 129,160,176
QK 228,196,148
v_exp_f32 v235, v235
QK 228,192,144
RT 84,247,320
CV 187,234,235
QC 232,192,168
ds_write_b128 v246, v[128:131] offset:0
QK 232,196,172
buffer_load_dwordx4 v237, s[40:43], s58 offen offset:3328 lds
QK 232,200,176
s_add_u32 s59, s68, 12288
CV 145,162,178
QK 232,204,180
RQ 204,239,44032
v_mul_f32 v228, s71, v228
QK 232,208,184
RQ 196,239,43008
v_mul_f32 v229, s71, v229
QK 232,212,188
RQ 200,238,44032
RQ 208,238,45056
PX 124,204,224
RQ 192,238,43008
CV 147,163,179
PX 128,208,224
RQ 212,239,45056
v_mul_f32 v230, s71, v230
PX 140,204,228
s_add_u32 m0, s60, 26368
ds_write_b128 v246, v[144:147] offset:1280
PX 144,208,228
s_waitcnt lgkmcnt(5)
v_mul_f32 v231, s71, v231
v_mul_f32 v233, s71, v233
PX 160,208,216
buffer_load_dwordx4 v237, s[44:47], s56 offen offset:256 lds
PX 156,204,216
v_exp_f32 v231, v231
PX 176,208,220
v_exp_f32 v230, v230
PX 172,204,220
v_exp_f32 v229, v229
PX 152,200,216
s_waitcnt vmcnt(23) lgkmcnt(1)
v_exp_f32 v228, v228
PX 148,196,216
s_barrier
PX 168,200,220
L_s19_%=:
RT 224,241,37888
v_mul_f32 v234, s71, v234
PX 164,196,220
CV 190,228,229
CV 191,230,231
QC 228,212,116
v_mul_f32 v232, s71, v232
v_mul_f32 v235, s71, v235
QK 228,208,112
L_c9_%=:
s_cbranch_execz L_c13_%=
v_exp_f32 v235, v235
QK 228,204,108
v_exp_f32 v234, v234
QK 228,200,104
v_exp_f32 v233, v233
QK 228,196,100
v_exp_f32 v232, v232
QK 228,192,96
CV 194,232,233
CV 195,234,235
QC 232,192,120
s_waitcnt vmcnt(41)
RQ 8,244,10240
QK 232,196,124
QK 232,200,128
RT 228,241,1024
QK 232,204,132
RT 226,241,44032
v_mul_f32 v229, s71, v229
QK 232,208,136
v_exp_f32 v229, v229
QK 232,212,140
RT 230,241,7168
v_mul_f32 v228, s71, v228
QC 248,212,164
RT 220,241,1040
v_mul_f32 v231, s71, v231
QK 248,208,160
s_add_u32 m0, s60, 74624
v_exp_f32 v231, v231
QK 248,204,156
v_exp_f32 v228, v228
QK 248,200,152
RT 216,241,37904
v_mul_f32 v230, s71, v230
QK 248,196,148
v_exp_f32 v230, v230
QK 248,192,144
CV 196,228,229
CV 197,230,231
QC 228,192,168
RQ 12,245,10240
QK 228,196,172
RQ 192,238,0
v_mul_f32 v233, s71, v233
QK 228,200,176
RT 222,241,7184
QK 228,204,180
QK 228,208,184
RT 218,241,44048
v_mul_f32 v234, s71, v234
QK 228,212,188
RQ 196,239,0
v_mul_f32 v232, s71, v232
v_smfmac_f32_16x16x64_bf16 a[232:235], a[236:239], v[180:187], v236
s_waitcnt lgkmcnt(4)
v_exp_f32 v234, v234
PW 96,212,224
buffer_store_dwordx2 a[16:17], v242, s[52:55], s59 offen offset:0 nt
RQ 200,238,1024
PW 96,180,228
v_exp_f32 v233, v233
PW 100,216,224
RQ 208,238,2048
v_mul_f32 v250, s71, v250
PW 100,184,228
buffer_store_dwordx2 a[18:19], v242, s[52:55], s59 offen offset:768 nt
RQ 212,239,2048
PW 104,220,224
RQ 204,239,1024
v_mul_f32 v249, s71, v249
PW 104,188,228
v_exp_f32 v232, v232
PW 108,224,224
)"
R"(
RT 224,240,38912
v_mul_f32 v235, s71, v235
PW 108,192,228
s_waitcnt lgkmcnt(4)
RT 226,240,45056
v_mul_f32 v251, s71, v251
PW 112,224,216
RT 230,240,8192
PW 112,192,220
RT 228,240,2048
CV 200,232,233
v_mfma_f32_16x16x32_bf16 a[252:255], v[220:223], a[216:219], a[252:255]
buffer_load_dwordx4 v237, s[48:51], s57 offen offset:128 lds
v_mfma_f32_16x16x32_bf16 a[252:255], v[188:191], a[220:223], a[252:255]
v_mul_f32 v248, s71, v248
v_mul_f32 v228, s71, v228
v_mfma_f32_16x16x32_bf16 a[248:251], v[216:219], a[216:219], a[248:251]
v_exp_f32 v250, v250
v_mfma_f32_16x16x32_bf16 a[248:251], v[184:187], a[220:223], a[248:251]
v_exp_f32 v248, v248
v_mfma_f32_16x16x32_bf16 a[244:247], v[212:215], a[216:219], a[244:247]
s_add_u32 s56, s63, 79872
v_mul_f32 v230, s71, v230
v_mfma_f32_16x16x32_bf16 a[244:247], v[180:183], a[220:223], a[244:247]
s_waitcnt lgkmcnt(0)
v_exp_f32 v249, v249
PW 116,212,224
s_add_u32 m0, s60, 75520
CV 204,248,249
v_mul_f32 v229, s71, v229
PW 116,180,228
v_exp_f32 v251, v251
PW 120,216,224
buffer_load_dwordx4 v237, s[48:51], s57 offen offset:256 lds
PW 120,184,228
s_add_u32 m0, s60, 86016
v_exp_f32 v235, v235
PW 124,220,224
RT 218,240,45072
CV 205,250,251
PW 124,188,228
RT 216,240,38928
PW 128,224,224
CV 201,234,235
PW 128,192,228
RT 220,240,2064
v_mul_f32 v231, s71, v231
QC 232,212,116
v_exp_f32 v230, v230
QK 232,208,112
v_exp_f32 v229, v229
QK 232,204,108
buffer_load_dwordx4 v237, s[48:51], s77 offen offset:0 lds
QK 232,200,104
s_add_u32 m0, s60, 36864
QK 232,196,100
v_exp_f32 v231, v231
QK 232,192,96
QC 248,192,120
v_exp_f32 v228, v228
QK 248,196,124
RT 222,240,8208
QK 248,200,128
RT 228,241,2048
CV 209,230,231
QK 248,204,132
v_mul_f32 v232, s71, v232
v_mul_f32 v235, s71, v235
QK 248,208,136
CV 208,228,229
v_mul_f32 v234, s71, v234
QK 248,212,140
buffer_load_dwordx4 v237, s[44:47], s56 offen offset:0 lds
QC 228,212,164
v_exp_f32 v232, v232
QK 228,208,160
v_exp_f32 v234, v234
QK 228,204,156
v_mul_f32 v233, s71, v233
QK 228,200,152
v_exp_f32 v233, v233
QK 228,196,148
v_exp_f32 v235, v235
QK 228,192,144
CV 198,232,233
CV 199,234,235
QC 232,192,168
buffer_store_dwordx2 a[20:21], v242, s[52:55], s59 offen offset:1536 nt
RT 224,241,38912
QK 232,196,172
RT 230,241,8192
QK 232,200,176
QK 232,204,180
RT 226,241,45056
v_mul_f32 v248, s71, v248
QK 232,208,184
RQ 192,238,6144
QK 232,212,188
RQ 204,239,7168
v_mul_f32 v251, s71, v251
v_smfmac_f32_16x16x64_bf16 a[232:235], a[240:243], v[188:195], v236
s_waitcnt lgkmcnt(4)
v_mul_f32 v249, s71, v249
v_mul_f32 v231, s71, v231
PW 144,224,216
v_mul_f32 v228, s71, v228
v_mul_f32 v229, s71, v229
PW 144,192,220
s_add_u32 m0, s60, 37760
RQ 208,238,8192
v_mul_f32 v250, s71, v250
PW 140,220,216
RQ 196,239,6144
PW 140,188,220
buffer_store_dwordx2 a[22:23], v242, s[52:55], s59 offen offset:2304 nt
v_mul_f32 v233, s71, v233
PW 136,216,216
RQ 212,239,8192
v_mul_f32 v232, s71, v232
PW 136,184,220
v_exp_f32 v248, v248
PW 132,212,216
RT 216,241,38928
v_mul_f32 v230, s71, v230
PW 132,180,220
s_waitcnt lgkmcnt(3)
RT 218,241,45072
PW 148,212,224
RT 222,241,8208
PW 148,180,228
RT 220,241,2064
v_mul_f32 v235, s71, v235
PW 152,216,224
buffer_load_dwordx4 v237, s[44:47], s56 offen offset:128 lds
PW 152,184,228
s_add_u32 m0, s60, 38656
v_exp_f32 v231, v231
PW 156,220,224
RQ 200,238,7168
v_mul_f32 v234, s71, v234
PW 156,188,228
v_exp_f32 v230, v230
PW 160,224,224
v_exp_f32 v251, v251
PW 160,192,228
s_waitcnt lgkmcnt(1)
v_exp_f32 v228, v228
PW 176,224,216
v_exp_f32 v229, v229
PW 176,192,220
s_add_u32 s59, s68, 18432
v_exp_f32 v250, v250
PW 172,220,216
buffer_load_dwordx4 v237, s[44:47], s56 offen offset:256 lds
PW 172,188,220
s_add_u32 m0, s60, 86912
v_exp_f32 v232, v232
PW 168,216,216
s_waitcnt vmcnt(21) lgkmcnt(0)
v_exp_f32 v233, v233
PW 168,184,220
s_barrier
PW 164,212,216
L_s20_%=:
CV 206,228,229
CV 207,230,231
PW 164,180,220
QC 228,212,116
CV 210,232,233
QK 228,208,112
v_exp_f32 v235, v235
QK 228,204,108
buffer_load_dwordx4 v237, s[48:51], s77 offen offset:128 lds
QK 228,200,104
v_exp_f32 v234, v234
QK 228,196,100
s_add_u32 m0, s60, 87808
RT 228,240,12288
CV 211,234,235
QK 228,192,96
RT 226,240,6144
CV 203,250,251
QC 232,192,120
s_waitcnt vmcnt(44)
v_exp_f32 v249, v249
QK 232,196,124
RT 230,240,18432
CV 202,248,249
QK 232,200,128
RT 218,240,6160
QK 232,204,132
v_mul_f32 v229, s71, v229
v_mul_f32 v231, s71, v231
QK 232,208,136
s_add_u32 s56, s63, 92160
v_mul_f32 v228, s71, v228
v_mul_f32 v230, s71, v230
QK 232,212,140
buffer_load_dwordx4 v237, s[48:51], s77 offen offset:256 lds
QC 248,212,164
v_exp_f32 v228, v228
QK 248,208,160
s_add_u32 s57, s63, 73728
v_exp_f32 v229, v229
QK 248,204,156
v_exp_f32 v230, v230
QK 248,200,152
RT 224,240,0
CV 212,228,229
QK 248,196,148
v_exp_f32 v231, v231
QK 248,192,144
RQ 16,244,12288
CV 213,230,231
QC 228,192,168
RT 216,240,16
v_mul_f32 v235, s71, v235
QK 228,196,172
buffer_store_dwordx2 a[24:25], v242, s[52:55], s59 offen offset:0 nt
QK 228,200,176
RT 220,240,12304
QK 228,204,180
RQ 204,239,13312
QK 228,208,184
RQ 20,245,12288
v_mul_f32 v234, s71, v234
QK 228,212,188
RT 222,240,18448
v_mul_f32 v232, s71, v232
v_smfmac_f32_16x16x64_bf16 a[232:235], a[236:239], v[196:203], v236
s_waitcnt lgkmcnt(4)
v_mul_f32 v251, s71, v251
PW 0,180,224
v_exp_f32 v235, v235
PW 0,196,228
v_exp_f32 v251, v251
PW 4,184,224
v_exp_f32 v234, v234
PW 4,200,228
RQ 212,239,14336
PW 8,188,224
buffer_store_dwordx2 a[26:27], v242, s[52:55], s59 offen offset:768 nt
v_mul_f32 v230, s71, v230
PW 8,204,228
v_mul_f32 v248, s71, v248
PW 12,192,224
RT 224,241,0
v_mul_f32 v233, s71, v233
PW 12,208,228
RT 230,241,18432
v_mul_f32 v249, s71, v249
PW 28,192,216
s_waitcnt lgkmcnt(3)
RT 226,241,6144
v_mul_f32 v229, s71, v229
PW 28,208,220
RT 228,241,12288
v_mul_f32 v228, s71, v228
PW 24,188,216
RQ 208,238,14336
v_mul_f32 v250, s71, v250
PW 24,204,220
s_add_u32 s77, s63, 86016
v_exp_f32 v248, v248
PW 20,184,216
s_add_u32 m0, s60, 50944
RQ 200,238,13312
PW 20,200,220
buffer_load_dwordx4 v237, s[48:51], s57 offen offset:256 lds
PW 16,180,216
v_exp_f32 v228, v228
PW 16,196,220
s_waitcnt lgkmcnt(2)
v_exp_f32 v232, v232
PW 32,180,224
s_add_u32 s58, s70, 12288
RQ 192,238,12288
CV 217,234,235
PW 32,196,228
RQ 196,239,12288
PW 36,184,224
v_exp_f32 v229, v229
PW 36,200,228
v_exp_f32 v233, v233
PW 40,188,224
s_add_u32 m0, s61, 8192
v_exp_f32 v249, v249
PW 40,204,228
CV 216,232,233
CV 224,228,229
PW 44,192,224
buffer_load_dwordx4 v237, s[40:43], s58 offen offset:0 lds
PW 44,208,228
s_waitcnt lgkmcnt(0)
RT 220,241,12304
QC 232,212,116
s_add_u32 m0, s60, 0
v_exp_f32 v250, v250
QK 232,208,112
RT 218,241,6160
CV 220,248,249
QK 232,204,108
v_exp_f32 v230, v230
QK 232,200,104
RT 216,241,16
QK 232,196,100
v_mul_f32 v231, s71, v231
QK 232,192,96
RT 224,240,1024
CV 221,250,251
QC 248,192,120
v_exp_f32 v231, v231
QK 248,196,124
QK 248,200,128
buffer_load_dwordx4 v237, s[44:47], s56 offen offset:0 lds
QK 248,204,132
CV 225,230,231
v_mul_f32 v234, s71, v234
QK 248,208,136
s_add_u32 m0, s60, 896
v_mul_f32 v232, s71, v232
v_mul_f32 v235, s71, v235
QK 248,212,140
v_exp_f32 v232, v232
QC 228,212,164
v_exp_f32 v234, v234
QK 228,208,160
RT 228,240,13312
v_mul_f32 v233, s71, v233
QK 228,204,156
v_exp_f32 v235, v235
QK 228,200,152
v_exp_f32 v233, v233
QK 228,196,148
CV 214,232,233
QK 228,192,144
RT 222,241,18448
CV 215,234,235
QC 232,192,168
buffer_load_dwordx4 v237, s[44:47], s56 offen offset:128 lds
QK 232,196,172
RT 226,240,7168
v_mul_f32 v250, s71, v250
QK 232,200,176
RT 94,247,1600
v_mul_f32 v248, s71, v248
QK 232,204,180
RQ 196,239,18432
v_mul_f32 v249, s71, v249
QK 232,208,184
RT 230,240,19456
v_mul_f32 v231, s71, v231
QK 232,212,188
s_add_u32 m0, s61, 6144
RT 88,247,0
v_smfmac_f32_16x16x64_bf16 a[232:235], a[240:243], v[204:211], v236
s_waitcnt lgkmcnt(5)
v_exp_f32 v249, v249
PW 60,192,216
RQ 204,239,19456
PW 60,208,220
buffer_load_dwordx4 v237, s[40:43], s58 offen offset:3072 lds
PW 56,188,216
v_mul_f32 v230, s71, v230
PW 56,204,220
RQ 208,238,20480
v_mul_f32 v228, s71, v228
PW 52,184,216
RQ 212,239,20480
v_mul_f32 v232, s71, v232
PW 52,200,220
RQ 200,238,19456
PW 48,180,216
RT 216,240,1040
PW 48,196,220
s_waitcnt lgkmcnt(4)
RT 220,240,13328
v_mul_f32 v234, s71, v234
PW 64,180,224
RT 218,240,7184
v_mul_f32 v235, s71, v235
PW 64,196,228
s_add_u32 m0, s60, 1792
RT 222,240,19472
v_mul_f32 v229, s71, v229
PW 68,184,224
buffer_load_dwordx4 v237, s[44:47], s56 offen offset:256 lds
PW 68,200,228
RQ 192,238,18432
PW 72,188,224
v_exp_f32 v228, v228
PW 72,204,228
v_exp_f32 v234, v234
PW 76,192,224
v_exp_f32 v250, v250
PW 76,208,228
s_waitcnt lgkmcnt(1)
v_exp_f32 v229, v229
PW 92,192,216
v_exp_f32 v248, v248
)"
R"(
PW 92,208,220
v_exp_f32 v231, v231
PW 88,188,216
v_exp_f32 v230, v230
PW 88,204,220
CV 222,228,229
CV 223,230,231
PW 84,184,216
s_waitcnt vmcnt(16) lgkmcnt(0)
v_exp_f32 v235, v235
PW 84,200,220
s_barrier
PW 80,180,216
L_s21_%=:
RT 230,241,31744
v_mul_f32 v251, s71, v251
PW 80,196,220
v_mul_f32 v233, s71, v233
CV 227,234,235
QC 228,212,116
v_exp_f32 v233, v233
QK 228,208,112
s_add_u32 m0, s60, 50048
RT 224,241,13312
QK 228,204,108
v_exp_f32 v251, v251
QK 228,200,104
RT 218,241,19472
CV 218,248,249
QK 228,196,100
v_exp_f32 v232, v232
QK 228,192,96
CV 226,232,233
QC 232,192,120
RT 226,241,19456
QK 232,196,124
CV 219,250,251
QK 232,200,128
RT 220,241,25616
QK 232,204,132
v_mul_f32 v228, s71, v228
v_mul_f32 v230, s71, v230
QK 232,208,136
buffer_load_dwordx4 v237, s[48:51], s57 offen offset:128 lds
QK 232,212,140
RT 228,241,25600
v_mul_f32 v229, s71, v229
QC 248,212,164
v_exp_f32 v230, v230
QK 248,208,160
v_exp_f32 v229, v229
QK 248,204,156
v_mul_f32 v231, s71, v231
QK 248,200,152
v_exp_f32 v228, v228
QK 248,196,148
v_exp_f32 v231, v231
QK 248,192,144
s_add_u32 m0, s60, 49152
CV 180,228,229
CV 181,230,231
QC 228,192,168
RT 216,241,13328
QK 228,196,172
QK 228,200,176
buffer_store_dwordx2 a[28:29], v242, s[52:55], s59 offen offset:1536 nt
v_mul_f32 v233, s71, v233
QK 228,204,180
QK 228,208,184
RT 222,241,31760
v_mul_f32 v235, s71, v235
QK 228,212,188
RQ 192,238,24576
v_smfmac_f32_16x16x64_bf16 a[232:235], a[236:239], v[212:219], v236
s_waitcnt lgkmcnt(2)
v_exp_f32 v235, v235
PW 96,196,224
v_mul_f32 v250, s71, v250
PW 96,212,228
v_exp_f32 v233, v233
PW 100,200,224
RQ 212,239,26624
v_mul_f32 v234, s71, v234
PW 100,216,228
v_exp_f32 v250, v250
PW 104,204,224
buffer_store_dwordx2 a[30:31], v242, s[52:55], s59 offen offset:2304 nt
RQ 208,238,26624
PW 104,220,228
v_exp_f32 v234, v234
PW 108,208,224
RT 224,240,14336
PW 108,224,228
RT 226,240,20480
v_mul_f32 v251, s71, v251
PW 112,208,216
s_waitcnt lgkmcnt(3)
RT 228,240,26624
PW 112,224,220
RT 230,240,32768
v_mul_f32 v249, s71, v249
v_mfma_f32_16x16x32_bf16 a[252:255], v[204:207], a[216:219], a[252:255]
buffer_load_dwordx4 v237, s[48:51], s57 offen offset:0 lds
v_mfma_f32_16x16x32_bf16 a[252:255], v[220:223], a[220:223], a[252:255]
RQ 204,239,25600
v_mul_f32 v232, s71, v232
v_mfma_f32_16x16x32_bf16 a[248:251], v[200:203], a[216:219], a[248:251]
v_exp_f32 v232, v232
v_mfma_f32_16x16x32_bf16 a[248:251], v[216:219], a[220:223], a[248:251]
v_mul_f32 v248, s71, v248
v_mul_f32 v228, s71, v228
v_mfma_f32_16x16x32_bf16 a[244:247], v[196:199], a[216:219], a[244:247]
s_waitcnt lgkmcnt(2)
v_exp_f32 v251, v251
v_mfma_f32_16x16x32_bf16 a[244:247], v[212:215], a[220:223], a[244:247]
s_add_u32 m0, s60, 61440
s_add_u32 s56, s63, 104448
CV 185,234,235
PW 116,196,224
s_waitcnt lgkmcnt(1)
RT 220,240,26640
CV 189,250,251
PW 116,212,228
RQ 196,239,24576
CV 184,232,233
PW 120,200,224
RQ 200,238,25600
v_mul_f32 v229, s71, v229
PW 120,216,228
L_c10_%=:
s_cbranch_execz L_c14_%=
v_exp_f32 v249, v249
PW 124,204,224
buffer_load_dwordx4 v237, s[48:51], s77 offen offset:0 lds
PW 124,220,228
v_exp_f32 v229, v229
PW 128,208,224
s_add_u32 s59, s68, 0
v_exp_f32 v228, v228
PW 128,224,228
v_mul_f32 v231, s71, v231
QC 232,212,116
s_add_u32 m0, s60, 12288
v_exp_f32 v248, v248
QK 232,208,112
s_waitcnt lgkmcnt(0)
RT 216,240,14352
v_mul_f32 v230, s71, v230
QK 232,204,108
v_exp_f32 v230, v230
QK 232,200,104
v_exp_f32 v231, v231
QK 232,196,100
buffer_load_dwordx4 v237, s[44:47], s56 offen offset:0 lds
QK 232,192,96
CV 188,248,249
QC 248,192,120
s_waitcnt vmcnt(51)
CV 192,228,229
CV 193,230,231
QK 248,196,124
RT 222,240,32784
QK 248,200,128
RT 230,241,32768
QK 248,204,132
RT 218,240,20496
v_mul_f32 v232, s71, v232
QK 248,208,136
buffer_store_dwordx2 a[32:33], v242, s[52:55], s59 offen offset:128 nt
QK 248,212,140
v_exp_f32 v232, v232
QC 228,212,164
v_mul_f32 v234, s71, v234
v_mul_f32 v235, s71, v235
QK 228,208,160
s_add_u32 m0, s60, 13184
RT 226,241,20480
v_mul_f32 v233, s71, v233
QK 228,204,156
v_exp_f32 v235, v235
QK 228,200,152
v_exp_f32 v233, v233
QK 228,196,148
v_exp_f32 v234, v234
QK 228,192,144
CV 182,232,233
CV 183,234,235
QC 232,192,168
buffer_load_dwordx4 v237, s[44:47], s56 offen offset:128 lds
QK 232,196,172
RQ 24,244,14336
QK 232,200,176
RQ 28,245,14336
v_mul_f32 v250, s71, v250
QK 232,204,180
RT 228,241,26624
v_mul_f32 v228, s71, v228
QK 232,208,184
RT 224,241,14336
QK 232,212,188
RQ 208,238,32768
v_smfmac_f32_16x16x64_bf16 a[232:235], a[240:243], v[220:227], v236
s_waitcnt lgkmcnt(5)
RQ 192,238,30720
v_mul_f32 v231, s71, v231
PW 144,208,216
RQ 200,238,31744
v_mul_f32 v251, s71, v251
PW 144,224,220
RQ 196,239,30720
PW 140,204,216
RQ 204,239,31744
v_mul_f32 v232, s71, v232
PW 140,220,220
buffer_store_dwordx2 a[34:35], v242, s[52:55], s59 offen offset:896 nt
RQ 212,239,32768
PW 136,200,216
v_mul_f32 v229, s71, v229
v_mul_f32 v230, s71, v230
PW 136,216,220
v_mul_f32 v249, s71, v249
v_mul_f32 v233, s71, v233
PW 132,196,216
s_add_u32 m0, s60, 14080
RT 216,241,14352
v_mul_f32 v234, s71, v234
PW 132,212,220
s_waitcnt lgkmcnt(3)
RT 218,241,20496
PW 148,196,224
RT 222,241,32784
PW 148,212,228
RT 220,241,26640
v_mul_f32 v248, s71, v248
PW 152,200,224
v_mul_f32 v235, s71, v235
PW 152,216,228
v_exp_f32 v230, v230
PW 156,204,224
buffer_load_dwordx4 v237, s[44:47], s56 offen offset:256 lds
PW 156,220,228
v_exp_f32 v231, v231
PW 160,208,224
v_exp_f32 v249, v249
PW 160,224,228
s_waitcnt lgkmcnt(0)
v_exp_f32 v235, v235
PW 176,208,216
v_exp_f32 v248, v248
PW 176,224,220
v_exp_f32 v228, v228
PW 172,204,216
s_add_u32 m0, s60, 62336
v_exp_f32 v229, v229
PW 172,220,220
v_exp_f32 v251, v251
PW 168,200,216
s_waitcnt vmcnt(19)
v_exp_f32 v232, v232
PW 168,216,220
s_barrier
PW 164,196,216
L_s22_%=:
buffer_load_dwordx4 v237, s[48:51], s77 offen offset:128 lds
PW 164,212,220
CV 190,228,229
CV 191,230,231
QC 228,212,116
v_exp_f32 v234, v234
QK 228,208,112
RT 218,240,30736
CV 195,234,235
QK 228,204,108
v_exp_f32 v233, v233
QK 228,200,104
s_add_u32 s58, s70, 12288
v_exp_f32 v250, v250
QK 228,196,100
s_add_u32 s56, s63, 116736
RT 228,240,36864
CV 194,232,233
QK 228,192,96
CV 187,250,251
QC 232,192,120
RT 216,240,24592
QK 232,196,124
RT 220,240,36880
QK 232,200,128
buffer_store_dwordx2 a[36:37], v242, s[52:55], s59 offen offset:1664 nt
QK 232,204,132
v_mul_f32 v228, s71, v228
v_mul_f32 v231, s71, v231
QK 232,208,136
s_add_u32 m0, s60, 63232
v_exp_f32 v228, v228
QK 232,212,140
CV 186,248,249
v_mul_f32 v229, s71, v229
QC 248,212,164
RT 224,240,24576
v_mul_f32 v230, s71, v230
QK 248,208,160
v_exp_f32 v229, v229
QK 248,204,156
v_exp_f32 v230, v230
QK 248,200,152
v_exp_f32 v231, v231
QK 248,196,148
RT 226,240,30720
CV 196,228,229
QK 248,192,144
RT 230,240,43008
CV 197,230,231
QC 228,192,168
buffer_load_dwordx4 v237, s[48:51], s77 offen offset:256 lds
QK 228,196,172
RT 222,240,43024
QK 228,200,176
RQ 200,238,37888
QK 228,204,180
s_add_u32 s57, s63, 98304
v_mul_f32 v232, s71, v232
v_mul_f32 v250, s71, v250
QK 228,208,184
RT 92,247,320
QK 228,212,188
v_mul_f32 v233, s71, v233
v_mul_f32 v248, s71, v248
v_smfmac_f32_16x16x64_bf16 a[232:235], a[236:239], v[180:187], v236
s_waitcnt lgkmcnt(3)
v_exp_f32 v233, v233
PW 0,212,224
s_add_u32 m0, s60, 75520
PW 0,180,228
RQ 212,239,38912
v_mul_f32 v234, s71, v234
PW 4,216,224
v_mul_f32 v251, s71, v251
PW 4,184,228
buffer_store_dwordx2 a[38:39], v242, s[52:55], s59 offen offset:2432 nt
RQ 208,238,38912
PW 8,220,224
v_exp_f32 v251, v251
PW 8,188,228
v_exp_f32 v232, v232
PW 12,224,224
RT 226,241,30720
PW 12,192,228
RT 230,241,43008
v_mul_f32 v235, s71, v235
PW 28,224,216
s_waitcnt lgkmcnt(3)
RT 224,241,24576
PW 28,192,220
RT 228,241,36864
v_mul_f32 v230, s71, v230
PW 24,220,216
v_exp_f32 v235, v235
PW 24,188,220
RQ 196,239,36864
PW 20,216,216
RQ 192,238,36864
CV 200,232,233
PW 20,184,220
buffer_load_dwordx4 v237, s[48:51], s57 offen offset:256 lds
PW 16,212,216
RQ 204,239,37888
v_mul_f32 v231, s71, v231
PW 16,180,220
s_waitcnt lgkmcnt(3)
RT 222,241,43024
v_mul_f32 v249, s71, v249
PW 32,212,224
v_exp_f32 v234, v234
PW 32,180,228
v_exp_f32 v248, v248
PW 36,216,224
v_exp_f32 v231, v231
PW 36,184,228
s_add_u32 m0, s61, 10112
v_exp_f32 v250, v250
PW 40,220,224
CV 201,234,235
PW 40,188,228
v_exp_f32 v249, v249
PW 44,224,224
RT 218,241,30736
PW 44,192,228
buffer_load_dwordx4 v237, s[40:43], s58 offen offset:128 lds
QC 232,212,116
v_mul_f32 v228, s71, v228
v_mul_f32 v229, s71, v229
QK 232,208,112
s_waitcnt lgkmcnt(1)
v_exp_f32 v230, v230
QK 232,204,108
s_add_u32 m0, s60, 24576
CV 204,248,249
CV 205,250,251
QK 232,200,104
)"
R"(
RT 220,241,36880
CV 209,230,231
QK 232,196,100
v_exp_f32 v229, v229
QK 232,192,96
v_exp_f32 v228, v228
QC 248,192,120
s_waitcnt vmcnt(50)
CV 208,228,229
QK 248,196,124
RT 226,240,31744
QK 248,200,128
buffer_load_dwordx4 v237, s[44:47], s56 offen offset:0 lds
QK 248,204,132
RT 224,240,25600
v_mul_f32 v233, s71, v233
QK 248,208,136
v_mul_f32 v232, s71, v232
v_mul_f32 v234, s71, v234
QK 248,212,140
RQ 36,245,4096
v_mul_f32 v235, s71, v235
QC 228,212,164
s_add_u32 m0, s60, 25472
v_exp_f32 v232, v232
QK 228,208,160
v_exp_f32 v233, v233
QK 228,204,156
v_exp_f32 v235, v235
QK 228,200,152
v_exp_f32 v234, v234
QK 228,196,148
RQ 32,244,4096
CV 198,232,233
QK 228,192,144
RT 216,241,24592
CV 199,234,235
QC 232,192,168
buffer_load_dwordx4 v237, s[44:47], s56 offen offset:128 lds
QK 232,196,172
RT 230,240,44032
QK 232,200,176
RQ 200,238,44032
QK 232,204,180
s_add_u32 m0, s61, 8064
RQ 196,239,43008
v_mul_f32 v230, s71, v230
QK 232,208,184
RT 228,240,37888
v_mul_f32 v248, s71, v248
QK 232,212,188
RQ 192,238,43008
v_mul_f32 v228, s71, v228
v_smfmac_f32_16x16x64_bf16 a[232:235], a[240:243], v[188:195], v236
s_waitcnt lgkmcnt(5)
v_exp_f32 v228, v228
PW 60,224,216
RQ 212,239,45056
PW 60,192,220
v_mul_f32 v251, s71, v251
PW 56,220,216
RQ 208,238,45056
v_mul_f32 v234, s71, v234
PW 56,188,220
buffer_load_dwordx4 v237, s[40:43], s58 offen offset:3200 lds
PW 52,216,216
s_add_u32 m0, s60, 26368
v_exp_f32 v230, v230
PW 52,184,220
v_exp_f32 v248, v248
PW 48,212,216
RT 218,240,31760
v_mul_f32 v229, s71, v229
PW 48,180,220
RT 216,240,25616
v_mul_f32 v235, s71, v235
PW 64,212,224
s_waitcnt lgkmcnt(2)
RT 220,240,37904
v_mul_f32 v231, s71, v231
PW 64,180,228
RT 222,240,44048
v_mul_f32 v233, s71, v233
PW 68,216,224
RQ 204,239,44032
v_mul_f32 v232, s71, v232
PW 68,184,228
v_mul_f32 v249, s71, v249
PW 72,220,224
v_exp_f32 v232, v232
PW 72,188,228
buffer_load_dwordx4 v237, s[44:47], s56 offen offset:256 lds
PW 76,224,224
v_exp_f32 v233, v233
PW 76,192,228
s_waitcnt lgkmcnt(1)
v_exp_f32 v231, v231
PW 92,224,216
v_exp_f32 v249, v249
PW 92,192,220
v_exp_f32 v229, v229
PW 88,220,216
CV 207,230,231
PW 88,188,220
s_add_u32 m0, s60, 74624
v_exp_f32 v234, v234
PW 84,216,216
s_waitcnt vmcnt(19) lgkmcnt(0)
v_mul_f32 v250, s71, v250
PW 84,184,220
s_barrier
PW 80,212,216
L_s23_%=:
buffer_load_dwordx4 v237, s[48:51], s57 offen offset:128 lds
PW 80,180,220
RT 230,241,7168
CV 206,228,229
QC 228,212,116
s_add_u32 s56, s63, 129024
v_exp_f32 v235, v235
QK 228,208,112
QK 228,204,108
s_add_u32 m0, s60, 73728
v_exp_f32 v250, v250
QK 228,200,104
CV 210,232,233
QK 228,196,100
RT 216,241,37904
CV 211,234,235
QK 228,192,96
s_add_u32 s59, s68, 6144
v_exp_f32 v251, v251
QC 232,192,120
buffer_load_dwordx4 v237, s[48:51], s57 offen offset:0 lds
QK 232,196,124
RT 226,241,44032
CV 203,250,251
QK 232,200,128
s_add_u32 s77, s63, 110592
CV 202,248,249
QK 232,204,132
buffer_store_dwordx2 a[40:41], v242, s[52:55], s59 offen offset:128 nt
v_mul_f32 v231, s71, v231
QK 232,208,136
s_add_u32 m0, s60, 86016
v_mul_f32 v228, s71, v228
v_mul_f32 v230, s71, v230
QK 232,212,140
RT 224,241,37888
v_mul_f32 v229, s71, v229
QC 248,212,164
v_exp_f32 v228, v228
QK 248,208,160
v_exp_f32 v231, v231
QK 248,204,156
v_exp_f32 v230, v230
QK 248,200,152
v_exp_f32 v229, v229
QK 248,196,148
CV 213,230,231
v_mul_f32 v233, s71, v233
QK 248,192,144
RT 228,241,1024
CV 212,228,229
QC 228,192,168
buffer_load_dwordx4 v237, s[48:51], s77 offen offset:0 lds
QK 228,196,172
v_exp_f32 v233, v233
QK 228,200,176
RQ 192,238,0
QK 228,204,180
RT 218,241,44048
v_mul_f32 v232, s71, v232
QK 228,208,184
RT 220,241,1040
QK 228,212,188
RQ 200,238,1024
v_smfmac_f32_16x16x64_bf16 a[232:235], a[236:239], v[196:203], v236
s_waitcnt lgkmcnt(4)
RQ 212,239,2048
PW 96,180,224
v_mul_f32 v235, s71, v235
PW 96,196,228
RT 222,241,7184
v_mul_f32 v250, s71, v250
PW 100,184,224
buffer_store_dwordx2 a[42:43], v242, s[52:55], s59 offen offset:896 nt
v_mul_f32 v234, s71, v234
PW 100,200,228
RQ 208,238,2048
v_mul_f32 v249, s71, v249
PW 104,188,224
s_add_u32 m0, s60, 36864
v_exp_f32 v234, v234
PW 104,204,228
v_exp_f32 v232, v232
PW 108,192,224
RT 224,240,38912
PW 108,208,228
s_waitcnt lgkmcnt(2)
RT 226,240,45056
v_mul_f32 v248, s71, v248
PW 112,192,216
RT 230,240,8192
v_mul_f32 v230, s71, v230
PW 112,208,220
RT 228,240,2048
CV 216,232,233
v_mfma_f32_16x16x32_bf16 a[252:255], v[188:191], a[216:219], a[252:255]
v_exp_f32 v250, v250
v_mfma_f32_16x16x32_bf16 a[252:255], v[204:207], a[220:223], a[252:255]
buffer_load_dwordx4 v237, s[44:47], s56 offen offset:0 lds
v_mfma_f32_16x16x32_bf16 a[248:251], v[184:187], a[216:219], a[248:251]
s_add_u32 m0, s60, 37760
RQ 204,239,1024
v_mul_f32 v251, s71, v251
v_mfma_f32_16x16x32_bf16 a[248:251], v[200:203], a[220:223], a[248:251]
v_mfma_f32_16x16x32_bf16 a[244:247], v[180:183], a[216:219], a[244:247]
v_exp_f32 v248, v248
v_mfma_f32_16x16x32_bf16 a[244:247], v[196:199], a[220:223], a[244:247]
s_waitcnt lgkmcnt(1)
v_exp_f32 v251, v251
PW 116,180,224
v_exp_f32 v235, v235
PW 116,196,228
RT 220,240,2064
v_mul_f32 v229, s71, v229
PW 120,184,224
RQ 196,239,0
CV 217,234,235
PW 120,200,228
v_exp_f32 v230, v230
PW 124,188,224
CV 221,250,251
v_mul_f32 v228, s71, v228
PW 124,204,228
buffer_load_dwordx4 v237, s[44:47], s56 offen offset:128 lds
PW 128,192,224
v_exp_f32 v229, v229
PW 128,208,228
QC 232,212,116
s_add_u32 m0, s60, 38656
RT 218,240,45072
QK 232,208,112
s_waitcnt lgkmcnt(1)
v_mul_f32 v231, s71, v231
QK 232,204,108
v_exp_f32 v228, v228
QK 232,200,104
v_exp_f32 v231, v231
QK 232,196,100
v_exp_f32 v249, v249
QK 232,192,96
CV 220,248,249
CV 225,230,231
QC 248,192,120
buffer_store_dwordx2 a[44:45], v242, s[52:55], s59 offen offset:1664 nt
RT 224,241,38912
QK 248,196,124
RT 230,241,8192
QK 248,200,128
buffer_load_dwordx4 v237, s[44:47], s56 offen offset:256 lds
QK 248,204,132
CV 224,228,229
v_mul_f32 v234, s71, v234
QK 248,208,136
RT 228,241,2048
v_mul_f32 v235, s71, v235
QK 248,212,140
v_exp_f32 v235, v235
QC 228,212,164
v_exp_f32 v234, v234
QK 228,208,160
RT 222,240,8208
v_mul_f32 v232, s71, v232
QK 228,204,156
s_add_u32 s57, s63, 122880
v_exp_f32 v232, v232
QK 228,200,152
RT 216,240,38928
v_mul_f32 v233, s71, v233
QK 228,196,148
v_exp_f32 v233, v233
QK 228,192,144
CV 214,232,233
CV 215,234,235
QC 232,192,168
buffer_store_dwordx2 a[46:47], v242, s[52:55], s59 offen offset:2432 nt
RT 226,241,45056
QK 232,196,172
v_mul_f32 v250, s71, v250
v_mul_f32 v251, s71, v251
QK 232,200,176
RQ 200,238,7168
QK 232,204,180
QK 232,208,184
RQ 208,238,8192
v_mul_f32 v248, s71, v248
QK 232,212,188
v_exp_f32 v250, v250
v_smfmac_f32_16x16x64_bf16 a[232:235], a[240:243], v[204:211], v236
s_waitcnt lgkmcnt(2)
RQ 204,239,7168
v_mul_f32 v249, s71, v249
PW 144,192,216
v_mul_f32 v230, s71, v230
PW 144,208,220
s_add_u32 m0, s60, 86912
v_exp_f32 v251, v251
PW 140,188,216
v_mul_f32 v231, s71, v231
PW 140,204,220
RQ 192,238,6144
v_mul_f32 v234, s71, v234
PW 136,184,216
v_exp_f32 v230, v230
PW 136,200,220
v_exp_f32 v249, v249
PW 132,180,216
s_add_u32 s56, s63, 141312
RT 218,241,45072
PW 132,196,220
RT 222,241,8208
v_mul_f32 v232, s71, v232
PW 148,180,224
RT 216,241,38928
v_mul_f32 v233, s71, v233
PW 148,196,228
s_add_u32 s59, s68, 12288
RT 220,241,2064
CV 219,250,251
PW 152,184,224
v_exp_f32 v248, v248
PW 152,200,228
RQ 196,239,6144
L_c11_%=:
s_cbranch_execz L_c15_%=
PW 156,188,224
v_exp_f32 v231, v231
PW 156,204,228
RQ 212,239,8192
v_mul_f32 v228, s71, v228
PW 160,192,224
v_mul_f32 v229, s71, v229
CV 223,230,231
PW 160,208,228
s_waitcnt lgkmcnt(2)
v_exp_f32 v232, v232
PW 176,192,216
s_add_u32 s58, s70, 12288
v_exp_f32 v233, v233
PW 176,208,220
v_exp_f32 v229, v229
PW 172,188,216
v_exp_f32 v228, v228
PW 172,204,220
CV 226,232,233
PW 168,184,216
s_waitcnt vmcnt(19) lgkmcnt(0)
PW 168,200,220
s_barrier
PW 164,180,216
L_s24_%=:
RT 224,240,0
CV 222,228,229
PW 164,196,220
v_exp_f32 v234, v234
QC 228,212,116
RT 218,240,6160
QK 228,208,112
v_mul_f32 v235, s71, v235
QK 228,204,108
QK 228,200,104
v_exp_f32 v235, v235
QK 228,196,100
CV 227,234,235
QK 228,192,96
QC 232,192,120
s_waitcnt vmcnt(51)
RT 226,240,6144
QK 232,196,124
RT 230,240,18432
QK 232,200,128
CV 218,248,249
QK 232,204,132
RQ 44,245,6144
v_mul_f32 v231, s71, v231
QK 232,208,136
buffer_load_dwordx4 v237, s[48:51], s77 offen offset:128 lds
QK 232,212,140
v_mul_f32 v229, s71, v229
v_mul_f32 v230, s71, v230
QC 248,212,164
s_add_u32 m0, s60, 87808
v_exp_f32 v231, v231
QK 248,208,160
v_exp_f32 v229, v229
QK 248,204,156
v_exp_f32 v230, v230
QK 248,200,152
)"
R"(
v_mul_f32 v228, s71, v228
CV 181,230,231
QK 248,196,148
v_exp_f32 v228, v228
QK 248,192,144
RT 228,240,12288
CV 180,228,229
QC 228,192,168
RT 222,240,18448
QK 228,196,172
v_mul_f32 v234, s71, v234
QK 228,200,176
buffer_load_dwordx4 v237, s[48:51], s77 offen offset:256 lds
QK 228,204,180
RQ 196,239,12288
QK 228,208,184
RT 220,240,12304
v_mul_f32 v232, s71, v232
QK 228,212,188
RQ 192,238,12288
v_smfmac_f32_16x16x64_bf16 a[232:235], a[236:239], v[212:219], v236
s_waitcnt lgkmcnt(3)
buffer_store_dwordx2 a[48:49], v242, s[52:55], s59 offen offset:128 nt
v_mul_f32 v235, s71, v235
PW 0,196,224
RT 216,240,16
v_mul_f32 v250, s71, v250
PW 0,212,228
RQ 208,238,14336
v_mul_f32 v249, s71, v249
PW 4,200,224
v_exp_f32 v232, v232
PW 4,216,228
RQ 40,244,6144
v_mul_f32 v248, s71, v248
PW 8,204,224
buffer_store_dwordx2 a[50:51], v242, s[52:55], s59 offen offset:896 nt
RQ 200,238,13312
PW 8,220,228
v_exp_f32 v249, v249
PW 12,208,224
RT 224,241,0
PW 12,224,228
s_waitcnt lgkmcnt(3)
RT 226,241,6144
v_mul_f32 v229, s71, v229
PW 28,208,216
RT 228,241,12288
v_mul_f32 v230, s71, v230
PW 28,224,220
RT 230,241,18432
v_mul_f32 v233, s71, v233
PW 24,204,216
v_exp_f32 v250, v250
PW 24,220,220
s_add_u32 m0, s60, 50944
RQ 212,239,14336
v_mul_f32 v251, s71, v251
PW 20,200,216
PW 20,216,220
buffer_load_dwordx4 v237, s[48:51], s57 offen offset:256 lds
PW 16,196,216
v_exp_f32 v233, v233
PW 16,212,220
s_waitcnt lgkmcnt(1)
RT 216,241,16
v_mul_f32 v231, s71, v231
PW 32,196,224
v_exp_f32 v231, v231
PW 32,212,228
v_exp_f32 v235, v235
PW 36,200,224
RQ 204,239,13312
CV 184,232,233
PW 36,216,228
RT 222,241,18448
PW 40,204,224
v_exp_f32 v251, v251
PW 40,220,228
v_exp_f32 v234, v234
PW 44,208,224
s_add_u32 m0, s61, 12032
CV 185,234,235
v_mul_f32 v228, s71, v228
PW 44,224,228
s_waitcnt lgkmcnt(2)
buffer_load_dwordx4 v237, s[40:43], s58 offen offset:256 lds
QC 232,212,116
v_exp_f32 v229, v229
QK 232,208,112
s_waitcnt lgkmcnt(0)
RT 218,241,6160
QK 232,204,108
v_exp_f32 v248, v248
QK 232,200,104
v_exp_f32 v228, v228
QK 232,196,100
CV 188,248,249
CV 192,228,229
QK 232,192,96
s_add_u32 m0, s60, 0
RT 226,240,7168
CV 189,250,251
QC 248,192,120
v_exp_f32 v230, v230
QK 248,196,124
RT 220,241,12304
CV 193,230,231
QK 248,200,128
buffer_load_dwordx4 v237, s[44:47], s56 offen offset:0 lds
QK 248,204,132
RT 230,240,19456
v_mul_f32 v235, s71, v235
QK 248,208,136
s_add_u32 m0, s60, 896
RT 90,247,1280
v_mul_f32 v233, s71, v233
QK 248,212,140
v_mul_f32 v232, s71, v232
QC 228,212,164
RT 224,240,1024
v_mul_f32 v234, s71, v234
QK 228,208,160
v_exp_f32 v235, v235
QK 228,204,156
v_exp_f32 v233, v233
QK 228,200,152
v_exp_f32 v232, v232
QK 228,196,148
v_exp_f32 v234, v234
QK 228,192,144
CV 182,232,233
CV 183,234,235
QC 232,192,168
buffer_load_dwordx4 v237, s[44:47], s56 offen offset:128 lds
QK 232,196,172
RQ 196,239,18432
QK 232,200,176
v_mul_f32 v251, s71, v251
QK 232,204,180
v_mul_f32 v230, s71, v230
QK 232,208,184
v_exp_f32 v230, v230
QK 232,212,188
RQ 192,238,18432
v_smfmac_f32_16x16x64_bf16 a[232:235], a[240:243], v[220:227], v236
s_waitcnt lgkmcnt(2)
v_exp_f32 v251, v251
PW 60,208,216
s_add_u32 m0, s61, 9984
RQ 208,238,20480
PW 60,224,220
RT 228,240,13312
PW 56,204,216
buffer_load_dwordx4 v237, s[40:43], s58 offen offset:3328 lds
PW 56,220,220
RQ 200,238,19456
v_mul_f32 v234, s71, v234
PW 52,200,216
RQ 212,239,20480
v_mul_f32 v249, s71, v249
PW 52,216,220
v_mul_f32 v231, s71, v231
v_mul_f32 v233, s71, v233
PW 48,196,216
s_add_u32 m0, s60, 1792
RT 218,240,7184
v_mul_f32 v250, s71, v250
PW 48,212,220
RT 216,240,1040
v_mul_f32 v248, s71, v248
PW 64,196,224
s_waitcnt lgkmcnt(4)
RT 222,240,19472
v_mul_f32 v228, s71, v228
PW 64,212,228
RT 220,240,13328
v_mul_f32 v229, s71, v229
PW 68,200,224
v_exp_f32 v229, v229
PW 68,216,228
RQ 204,239,19456
v_mul_f32 v235, s71, v235
PW 72,204,224
buffer_load_dwordx4 v237, s[44:47], s56 offen offset:256 lds
PW 72,220,228
v_exp_f32 v249, v249
PW 76,208,224
s_waitcnt lgkmcnt(2)
v_exp_f32 v231, v231
PW 76,224,228
s_add_u32 s56, s63, 153600
v_exp_f32 v248, v248
PW 92,208,216
s_waitcnt lgkmcnt(1)
v_exp_f32 v228, v228
PW 92,224,220
v_exp_f32 v233, v233
PW 88,204,216
v_exp_f32 v235, v235
PW 88,220,220
s_add_u32 m0, s60, 50048
v_exp_f32 v234, v234
PW 84,200,216
s_waitcnt vmcnt(19) lgkmcnt(0)
CV 191,230,231
v_mul_f32 v232, s71, v232
PW 84,216,220
s_barrier
PW 80,196,216
L_s25_%=:
buffer_load_dwordx4 v237, s[48:51], s57 offen offset:128 lds
PW 80,212,220
RT 224,241,13312
CV 190,228,229
QC 228,212,116
s_add_u32 m0, s60, 49152
CV 195,234,235
QK 228,208,112
v_exp_f32 v232, v232
QK 228,204,108
RT 228,241,25600
CV 186,248,249
QK 228,200,104
QK 228,196,100
CV 194,232,233
QK 228,192,96
RT 222,241,31760
QC 232,192,120
v_exp_f32 v250, v250
QK 232,196,124
RT 230,241,31744
CV 187,250,251
QK 232,200,128
buffer_load_dwordx4 v237, s[48:51], s57 offen offset:0 lds
QK 232,204,132
RT 226,241,19456
v_mul_f32 v230, s71, v230
QK 232,208,136
s_add_u32 s77, s63, 135168
v_mul_f32 v229, s71, v229
v_mul_f32 v231, s71, v231
QK 232,212,140
v_mul_f32 v228, s71, v228
QC 248,212,164
v_exp_f32 v230, v230
QK 248,208,160
v_exp_f32 v229, v229
QK 248,204,156
v_exp_f32 v231, v231
QK 248,200,152
v_exp_f32 v228, v228
QK 248,196,148
RT 218,241,19472
v_mul_f32 v232, s71, v232
QK 248,192,144
CV 196,228,229
CV 197,230,231
QC 228,192,168
buffer_store_dwordx2 a[52:53], v242, s[52:55], s59 offen offset:1664 nt
v_mul_f32 v234, s71, v234
QK 228,196,172
RT 216,241,13328
QK 228,200,176
RT 220,241,25616
v_mul_f32 v235, s71, v235
QK 228,204,180
v_mul_f32 v251, s71, v251
QK 228,208,184
v_mul_f32 v249, s71, v249
QK 228,212,188
s_add_u32 m0, s60, 61440
RQ 200,238,25600
v_smfmac_f32_16x16x64_bf16 a[232:235], a[236:239], v[180:187], v236
s_waitcnt lgkmcnt(3)
v_exp_f32 v232, v232
PW 96,212,224
RQ 192,238,24576
PW 96,180,228
buffer_store_dwordx2 a[54:55], v242, s[52:55], s59 offen offset:2432 nt
PW 100,216,224
v_mul_f32 v233, s71, v233
PW 100,184,228
RQ 212,239,26624
PW 104,220,224
v_exp_f32 v235, v235
PW 104,188,228
v_exp_f32 v233, v233
PW 108,224,224
RT 224,240,14336
v_mul_f32 v231, s71, v231
PW 108,192,228
s_waitcnt lgkmcnt(2)
RT 226,240,20480
v_mul_f32 v248, s71, v248
PW 112,224,216
RT 230,240,32768
CV 200,232,233
PW 112,192,220
RT 228,240,26624
v_mul_f32 v229, s71, v229
v_mfma_f32_16x16x32_bf16 a[252:255], v[220:223], a[216:219], a[252:255]
buffer_load_dwordx4 v237, s[48:51], s77 offen offset:0 lds
v_mfma_f32_16x16x32_bf16 a[252:255], v[188:191], a[220:223], a[252:255]
RQ 196,239,24576
v_mul_f32 v250, s71, v250
v_mfma_f32_16x16x32_bf16 a[248:251], v[216:219], a[216:219], a[248:251]
v_exp_f32 v249, v249
v_mfma_f32_16x16x32_bf16 a[248:251], v[184:187], a[220:223], a[248:251]
RQ 208,238,26624
v_mfma_f32_16x16x32_bf16 a[244:247], v[212:215], a[216:219], a[244:247]
s_add_u32 s59, s68, 18432
RQ 204,239,25600
v_mul_f32 v228, s71, v228
v_mfma_f32_16x16x32_bf16 a[244:247], v[180:183], a[220:223], a[244:247]
s_waitcnt lgkmcnt(3)
v_exp_f32 v229, v229
PW 116,212,224
s_add_u32 m0, s60, 12288
v_exp_f32 v248, v248
PW 116,180,228
RT 218,240,20496
v_mul_f32 v230, s71, v230
PW 120,216,224
buffer_load_dwordx4 v237, s[44:47], s56 offen offset:0 lds
PW 120,184,228
v_exp_f32 v251, v251
PW 124,220,224
v_exp_f32 v234, v234
PW 124,188,228
CV 201,234,235
PW 128,224,224
CV 204,248,249
PW 128,192,228
s_add_u32 s58, s70, 18432
v_exp_f32 v228, v228
QC 232,212,116
s_waitcnt lgkmcnt(0)
v_exp_f32 v250, v250
QK 232,208,112
s_add_u32 m0, s60, 13184
RT 230,241,32768
QK 232,204,108
buffer_load_dwordx4 v237, s[44:47], s56 offen offset:128 lds
QK 232,200,104
v_exp_f32 v230, v230
QK 232,196,100
CV 205,250,251
QK 232,192,96
RT 222,240,32784
QC 248,192,120
s_waitcnt vmcnt(48)
RT 216,240,14352
CV 208,228,229
QK 248,196,124
s_add_u32 m0, s60, 14080
v_exp_f32 v231, v231
QK 248,200,128
RT 228,241,26624
CV 209,230,231
QK 248,204,132
RT 224,241,14336
QK 248,208,136
v_mul_f32 v233, s71, v233
v_mul_f32 v235, s71, v235
QK 248,212,140
buffer_load_dwordx4 v237, s[44:47], s56 offen offset:256 lds
QC 228,212,164
v_exp_f32 v235, v235
QK 228,208,160
v_mul_f32 v232, s71, v232
v_mul_f32 v234, s71, v234
QK 228,204,156
v_exp_f32 v233, v233
QK 228,200,152
v_exp_f32 v232, v232
QK 228,196,148
v_exp_f32 v234, v234
QK 228,192,144
CV 198,232,233
CV 199,234,235
QC 232,192,168
buffer_store_dwordx2 a[56:57], v242, s[52:55], s59 offen offset:128 nt
RT 220,240,26640
QK 232,196,172
RQ 48,244,8192
QK 232,200,176
RQ 200,238,31744
QK 232,204,180
RT 226,241,20480
QK 232,208,184
v_mul_f32 v251, s71, v251
QK 232,212,188
s_add_u32 m0, s60, 62336
v_smfmac_f32_16x16x64_bf16 a[232:235], a[240:243], v[188:195], v236
s_waitcnt lgkmcnt(3)
buffer_store_dwordx2 a[58:59], v242, s[52:55], s59 offen offset:896 nt
RQ 52,245,8192
PW 144,224,216
s_add_u32 s57, s63, 147456
)"
R"(
v_mul_f32 v230, s71, v230
PW 144,192,220
v_mul_f32 v231, s71, v231
PW 140,220,216
RQ 192,238,30720
v_mul_f32 v229, s71, v229
PW 140,188,220
v_mul_f32 v228, s71, v228
v_mul_f32 v235, s71, v235
PW 136,216,216
RQ 208,238,32768
v_mul_f32 v248, s71, v248
PW 136,184,220
RQ 204,239,31744
PW 132,212,216
RT 216,241,14352
v_mul_f32 v234, s71, v234
PW 132,180,220
s_waitcnt lgkmcnt(4)
RT 218,241,20496
v_mul_f32 v250, s71, v250
PW 148,212,224
RT 222,241,32784
v_mul_f32 v232, s71, v232
PW 148,180,228
RT 220,241,26640
v_mul_f32 v233, s71, v233
PW 152,216,224
RQ 196,239,30720
PW 152,184,228
v_exp_f32 v228, v228
PW 156,220,224
v_exp_f32 v231, v231
PW 156,188,228
RQ 212,239,32768
v_mul_f32 v249, s71, v249
PW 160,224,224
v_exp_f32 v235, v235
PW 160,192,228
s_waitcnt lgkmcnt(2)
v_exp_f32 v230, v230
PW 176,224,216
v_exp_f32 v229, v229
PW 176,192,220
v_exp_f32 v249, v249
PW 172,220,216
v_exp_f32 v251, v251
PW 172,188,220
v_exp_f32 v232, v232
PW 168,216,216
s_waitcnt vmcnt(19) lgkmcnt(0)
CV 206,228,229
CV 207,230,231
PW 168,184,220
s_barrier
PW 164,212,216
L_s26_%=:
buffer_load_dwordx4 v237, s[48:51], s77 offen offset:128 lds
PW 164,180,220
v_exp_f32 v233, v233
QC 228,212,116
v_exp_f32 v234, v234
QK 228,208,112
RT 216,240,24592
CV 210,232,233
QK 228,204,108
s_add_u32 m0, s60, 63232
QK 228,200,104
v_exp_f32 v248, v248
QK 228,196,100
v_exp_f32 v250, v250
QK 228,192,96
CV 203,250,251
CV 211,234,235
QC 232,192,120
RT 230,240,43008
CV 202,248,249
QK 232,196,124
buffer_load_dwordx4 v237, s[48:51], s77 offen offset:256 lds
QK 232,200,128
RT 224,240,24576
QK 232,204,132
v_mul_f32 v230, s71, v230
v_mul_f32 v231, s71, v231
QK 232,208,136
RT 226,240,30720
v_mul_f32 v229, s71, v229
QK 232,212,140
buffer_store_dwordx2 a[60:61], v242, s[52:55], s59 offen offset:1664 nt
v_mul_f32 v228, s71, v228
QC 248,212,164
v_exp_f32 v228, v228
QK 248,208,160
s_add_u32 m0, s60, 75520
v_exp_f32 v231, v231
QK 248,204,156
v_exp_f32 v230, v230
QK 248,200,152
buffer_load_dwordx4 v237, s[48:51], s57 offen offset:256 lds
QK 248,196,148
v_exp_f32 v229, v229
QK 248,192,144
CV 212,228,229
CV 213,230,231
QC 228,192,168
RT 228,240,36864
QK 228,196,172
s_add_u32 m0, s61, 14336
RT 222,240,43024
v_mul_f32 v235, s71, v235
QK 228,200,176
RT 220,240,36880
v_mul_f32 v234, s71, v234
QK 228,204,180
buffer_store_dwordx2 a[62:63], v242, s[52:55], s59 offen offset:2432 nt
RT 218,240,30736
QK 228,208,184
v_mul_f32 v232, s71, v232
QK 228,212,188
v_exp_f32 v232, v232
v_smfmac_f32_16x16x64_bf16 a[232:235], a[236:239], v[196:203], v236
s_waitcnt lgkmcnt(3)
buffer_load_dwordx4 v237, s[40:43], s58 offen offset:0 lds
PW 0,180,224
v_mul_f32 v233, s71, v233
v_mul_f32 v250, s71, v250
PW 0,196,228
PW 4,184,224
RQ 200,238,37888
v_mul_f32 v248, s71, v248
PW 4,200,228
RQ 204,239,37888
PW 8,188,224
s_add_u32 m0, s60, 24576
RQ 208,238,38912
PW 8,204,228
RQ 212,239,38912
v_mul_f32 v251, s71, v251
PW 12,192,224
RT 224,241,24576
v_mul_f32 v230, s71, v230
PW 12,208,228
s_waitcnt lgkmcnt(5)
L_c12_%=:
s_cbranch_execz L_c16_%=
RT 226,241,30720
v_mul_f32 v249, s71, v249
PW 28,192,216
RT 230,241,43008
v_mul_f32 v228, s71, v228
PW 28,208,220
s_add_u32 s56, s63, 165888
RT 228,241,36864
PW 24,188,216
PW 24,204,220
v_exp_f32 v235, v235
PW 20,184,216
v_exp_f32 v251, v251
PW 20,200,220
v_exp_f32 v250, v250
PW 16,180,216
v_exp_f32 v233, v233
PW 16,196,220
s_waitcnt lgkmcnt(0)
RQ 192,238,36864
v_mul_f32 v231, s71, v231
PW 32,180,224
v_exp_f32 v249, v249
PW 32,196,228
v_exp_f32 v231, v231
PW 36,184,224
RQ 196,239,36864
CV 216,232,233
PW 36,200,228
v_exp_f32 v234, v234
PW 40,188,224
CV 221,250,251
PW 40,204,228
CV 217,234,235
PW 44,192,224
v_exp_f32 v228, v228
PW 44,208,228
v_mul_f32 v229, s71, v229
QC 232,212,116
v_exp_f32 v230, v230
QK 232,208,112
RT 220,241,36880
CV 225,230,231
QK 232,204,108
v_exp_f32 v248, v248
QK 232,200,104
s_waitcnt lgkmcnt(1)
RT 228,240,37888
QK 232,196,100
CV 220,248,249
QK 232,192,96
RT 222,241,43024
QC 248,192,120
s_waitcnt vmcnt(37)
v_exp_f32 v229, v229
QK 248,196,124
RT 216,241,24592
QK 248,200,128
RT 224,240,25600
CV 224,228,229
QK 248,204,132
v_mul_f32 v233, s71, v233
v_mul_f32 v235, s71, v235
QK 248,208,136
RT 218,241,30736
v_mul_f32 v232, s71, v232
QK 248,212,140
v_mul_f32 v234, s71, v234
QC 228,212,164
v_exp_f32 v234, v234
QK 228,208,160
v_exp_f32 v235, v235
QK 228,204,156
v_exp_f32 v233, v233
QK 228,200,152
buffer_load_dwordx4 v237, s[44:47], s56 offen offset:0 lds
QK 228,196,148
s_add_u32 m0, s60, 25472
v_exp_f32 v232, v232
QK 228,192,144
CV 214,232,233
CV 215,234,235
QC 232,192,168
RT 230,240,44032
QK 232,196,172
QK 232,200,176
RQ 56,244,10240
QK 232,204,180
QK 232,208,184
RQ 196,239,43008
QK 232,212,188
RT 226,240,31744
v_mul_f32 v228, s71, v228
v_smfmac_f32_16x16x64_bf16 a[232:235], a[240:243], v[204:211], v236
s_waitcnt lgkmcnt(3)
RQ 212,239,45056
PW 60,192,216
buffer_load_dwordx4 v237, s[44:47], s56 offen offset:128 lds
PW 60,208,220
v_exp_f32 v228, v228
PW 56,188,216
RQ 192,238,43008
v_mul_f32 v251, s71, v251
PW 56,204,220
RQ 208,238,45056
v_mul_f32 v248, s71, v248
PW 52,184,216
RQ 60,245,10240
v_mul_f32 v232, s71, v232
PW 52,200,220
v_exp_f32 v248, v248
PW 48,180,216
RT 216,240,25616
v_mul_f32 v250, s71, v250
PW 48,196,220
s_waitcnt lgkmcnt(4)
RT 220,240,37904
v_mul_f32 v233, s71, v233
PW 64,180,224
s_add_u32 m0, s61, 12288
RT 218,240,31760
PW 64,196,228
RT 222,240,44048
v_mul_f32 v249, s71, v249
PW 68,184,224
buffer_load_dwordx4 v237, s[40:43], s58 offen offset:3072 lds
PW 68,200,228
RQ 204,239,44032
v_mul_f32 v229, s71, v229
PW 72,188,224
RQ 200,238,44032
v_mul_f32 v230, s71, v230
PW 72,204,228
v_exp_f32 v229, v229
PW 76,192,224
s_waitcnt lgkmcnt(3)
v_mul_f32 v231, s71, v231
v_mul_f32 v235, s71, v235
PW 76,208,228
s_add_u32 m0, s60, 26368
v_exp_f32 v233, v233
PW 92,192,216
s_waitcnt lgkmcnt(2)
v_exp_f32 v232, v232
PW 92,208,220
CV 222,228,229
v_mul_f32 v234, s71, v234
PW 88,188,216
buffer_load_dwordx4 v237, s[44:47], s56 offen offset:256 lds
PW 88,204,220
v_exp_f32 v230, v230
PW 84,184,216
s_waitcnt vmcnt(19) lgkmcnt(0)
v_exp_f32 v231, v231
PW 84,200,220
s_barrier
PW 80,180,216
L_s27_%=:
v_exp_f32 v235, v235
PW 80,196,220
CV 223,230,231
CV 226,232,233
QC 228,212,116
v_exp_f32 v251, v251
QK 228,208,112
s_add_u32 s59, s68, 0
v_exp_f32 v249, v249
QK 228,204,108
s_add_u32 m0, s60, 74624
CV 218,248,249
QK 228,200,104
buffer_load_dwordx4 v237, s[48:51], s57 offen offset:128 lds
QK 228,196,100
v_exp_f32 v234, v234
QK 228,192,96
s_add_u32 s77, s63, 159744
RT 226,241,44032
CV 227,234,235
QC 232,192,120
v_exp_f32 v250, v250
QK 232,196,124
RT 228,241,1024
CV 219,250,251
QK 232,200,128
buffer_store_dwordx2 a[64:65], v242, s[52:55], s59 offen offset:256 nt
RT 218,241,44048
QK 232,204,132
RT 230,241,7168
v_mul_f32 v231, s71, v231
QK 232,208,136
v_exp_f32 v231, v231
QK 232,212,140
s_add_u32 m0, s60, 73728
v_mul_f32 v229, s71, v229
v_mul_f32 v230, s71, v230
QC 248,212,164
RT 220,241,1040
v_mul_f32 v228, s71, v228
QK 248,208,160
v_exp_f32 v229, v229
QK 248,204,156
v_exp_f32 v228, v228
QK 248,200,152
v_exp_f32 v230, v230
QK 248,196,148
CV 180,228,229
CV 181,230,231
QK 248,192,144
RT 224,241,37888
v_mul_f32 v235, s71, v235
QC 228,192,168
buffer_load_dwordx4 v237, s[48:51], s57 offen offset:0 lds
QK 228,196,172
QK 228,200,176
RT 216,241,37904
QK 228,204,180
v_mul_f32 v234, s71, v234
QK 228,208,184
QK 228,212,188
s_add_u32 s57, s63, 172032
RT 222,241,7184
v_smfmac_f32_16x16x64_bf16 a[232:235], a[236:239], v[212:219], v236
s_waitcnt lgkmcnt(2)
RQ 192,238,0
v_mul_f32 v249, s71, v249
PW 96,196,224
buffer_store_dwordx2 a[66:67], v242, s[52:55], s59 offen offset:1024 nt
PW 96,212,228
RQ 200,238,1024
v_mul_f32 v233, s71, v233
PW 100,200,224
v_exp_f32 v235, v235
PW 100,216,228
v_exp_f32 v234, v234
PW 104,204,224
v_mul_f32 v250, s71, v250
PW 104,220,228
RQ 196,239,0
v_mul_f32 v228, s71, v228
PW 108,208,224
RT 224,240,38912
v_mul_f32 v248, s71, v248
PW 108,224,228
s_waitcnt lgkmcnt(2)
RT 226,240,45056
PW 112,208,216
s_add_u32 s56, s63, 178176
RT 230,240,8192
v_mul_f32 v251, s71, v251
PW 112,224,220
s_add_u32 m0, s60, 86016
RT 228,240,2048
v_mul_f32 v232, s71, v232
v_mfma_f32_16x16x32_bf16 a[252:255], v[204:207], a[216:219], a[252:255]
buffer_load_dwordx4 v237, s[48:51], s77 offen offset:0 lds
)"
R"(
v_mfma_f32_16x16x32_bf16 a[252:255], v[220:223], a[220:223], a[252:255]
v_exp_f32 v250, v250
v_mfma_f32_16x16x32_bf16 a[248:251], v[200:203], a[216:219], a[248:251]
v_exp_f32 v248, v248
v_mfma_f32_16x16x32_bf16 a[248:251], v[216:219], a[220:223], a[248:251]
s_add_u32 m0, s60, 36864
RQ 204,239,1024
v_mfma_f32_16x16x32_bf16 a[244:247], v[196:199], a[216:219], a[244:247]
RQ 208,238,2048
v_mul_f32 v230, s71, v230
v_mfma_f32_16x16x32_bf16 a[244:247], v[212:215], a[220:223], a[244:247]
s_waitcnt lgkmcnt(2)
v_exp_f32 v232, v232
PW 116,196,224
RQ 212,239,2048
v_mul_f32 v231, s71, v231
PW 116,212,228
v_exp_f32 v233, v233
PW 120,200,224
buffer_load_dwordx4 v237, s[44:47], s56 offen offset:0 lds
PW 120,216,228
CV 184,232,233
v_mul_f32 v229, s71, v229
PW 124,204,224
s_add_u32 m0, s60, 37760
RT 216,240,38928
CV 185,234,235
PW 124,220,228
v_exp_f32 v229, v229
PW 128,208,224
v_exp_f32 v230, v230
PW 128,224,228
s_waitcnt lgkmcnt(1)
v_exp_f32 v231, v231
QC 232,212,116
v_exp_f32 v228, v228
QK 232,208,112
v_exp_f32 v249, v249
QK 232,204,108
v_exp_f32 v251, v251
QK 232,200,104
buffer_load_dwordx4 v237, s[44:47], s56 offen offset:128 lds
QK 232,196,100
s_add_u32 m0, s60, 38656
RT 220,240,2064
CV 189,250,251
QK 232,192,96
CV 188,248,249
QC 248,192,120
RT 218,240,45072
QK 248,196,124
RT 230,241,8192
CV 192,228,229
QK 248,200,128
RT 228,241,2048
QK 248,204,132
v_mul_f32 v232, s71, v232
v_mul_f32 v234, s71, v234
QK 248,208,136
CV 193,230,231
v_mul_f32 v233, s71, v233
QK 248,212,140
RT 224,241,38912
v_mul_f32 v235, s71, v235
QC 228,212,164
buffer_load_dwordx4 v237, s[44:47], s56 offen offset:256 lds
QK 228,208,160
v_exp_f32 v233, v233
QK 228,204,156
v_exp_f32 v235, v235
QK 228,200,152
v_exp_f32 v234, v234
QK 228,196,148
v_exp_f32 v232, v232
QK 228,192,144
CV 182,232,233
CV 183,234,235
QC 232,192,168
RT 222,240,8208
QK 232,196,172
buffer_store_dwordx2 a[68:69], v242, s[52:55], s59 offen offset:1792 nt
QK 232,200,176
v_mul_f32 v249, s71, v249
QK 232,204,180
RQ 200,238,7168
v_mul_f32 v229, s71, v229
QK 232,208,184
v_mul_f32 v230, s71, v230
QK 232,212,188
v_exp_f32 v230, v230
v_smfmac_f32_16x16x64_bf16 a[232:235], a[240:243], v[220:227], v236
s_waitcnt lgkmcnt(1)
buffer_store_dwordx2 a[70:71], v242, s[52:55], s59 offen offset:2560 nt
RQ 208,238,8192
PW 144,208,216
RT 226,241,45056
PW 144,224,220
RQ 196,239,6144
PW 140,204,216
RQ 204,239,7168
v_mul_f32 v248, s71, v248
PW 140,220,220
v_mul_f32 v228, s71, v228
v_mul_f32 v231, s71, v231
PW 136,200,216
v_exp_f32 v231, v231
PW 136,216,220
RQ 212,239,8192
PW 132,196,216
RT 218,241,45072
v_mul_f32 v234, s71, v234
PW 132,212,220
s_waitcnt lgkmcnt(3)
RT 222,241,8208
v_mul_f32 v232, s71, v232
PW 148,196,224
RT 216,241,38928
v_mul_f32 v250, s71, v250
PW 148,212,228
s_add_u32 m0, s60, 86912
RT 220,241,2064
v_mul_f32 v235, s71, v235
PW 152,200,224
v_exp_f32 v234, v234
PW 152,216,228
v_exp_f32 v235, v235
PW 156,204,224
RQ 192,238,6144
v_mul_f32 v251, s71, v251
PW 156,220,228
v_exp_f32 v232, v232
PW 160,208,224
v_exp_f32 v251, v251
PW 160,224,228
s_waitcnt lgkmcnt(1)
CV 191,230,231
PW 176,208,216
PW 176,224,220
v_exp_f32 v228, v228
PW 172,204,216
v_exp_f32 v248, v248
PW 172,220,220
v_exp_f32 v250, v250
PW 168,200,216
s_waitcnt vmcnt(19) lgkmcnt(0)
v_exp_f32 v229, v229
PW 168,216,220
s_barrier
PW 164,196,216
L_s28_%=:
buffer_load_dwordx4 v237, s[48:51], s77 offen offset:128 lds
PW 164,212,220
CV 190,228,229
CV 195,234,235
QC 228,212,116
s_add_u32 m0, s60, 87808
s_add_u32 s59, s68, 6144
v_mul_f32 v233, s71, v233
QK 228,208,112
v_exp_f32 v233, v233
QK 228,204,108
v_exp_f32 v249, v249
QK 228,200,104
RT 226,240,6144
CV 194,232,233
QK 228,196,100
QK 228,192,96
RT 222,240,18448
CV 186,248,249
QC 232,192,120
s_waitcnt vmcnt(32)
RT 228,240,12288
QK 232,196,124
RT 220,240,12304
CV 187,250,251
QK 232,200,128
buffer_load_dwordx4 v237, s[48:51], s77 offen offset:256 lds
QK 232,204,132
RT 230,240,18432
v_mul_f32 v228, s71, v228
QK 232,208,136
v_exp_f32 v228, v228
QK 232,212,140
RT 224,240,0
QC 248,212,164
v_mul_f32 v229, s71, v229
v_mul_f32 v230, s71, v230
QK 248,208,160
RT 216,240,16
v_mul_f32 v231, s71, v231
QK 248,204,156
v_exp_f32 v229, v229
QK 248,200,152
s_add_u32 m0, s60, 50944
v_exp_f32 v231, v231
QK 248,196,148
s_mov_b32 s56, s66
v_exp_f32 v230, v230
QK 248,192,144
CV 196,228,229
CV 197,230,231
QC 228,192,168
buffer_load_dwordx4 v237, s[48:51], s57 offen offset:256 lds
QK 228,196,172
QK 228,200,176
v_mul_f32 v234, s71, v234
v_mul_f32 v235, s71, v235
QK 228,204,180
s_add_u32 s58, s70, 18432
RQ 64,244,12288
v_mul_f32 v233, s71, v233
QK 228,208,184
v_exp_f32 v234, v234
QK 228,212,188
RQ 68,245,12288
v_mul_f32 v232, s71, v232
v_smfmac_f32_16x16x64_bf16 a[232:235], a[236:239], v[180:187], v236
s_waitcnt lgkmcnt(2)
RT 218,240,6160
PW 0,212,224
v_exp_f32 v233, v233
PW 0,180,228
buffer_store_dwordx2 a[72:73], v242, s[52:55], s59 offen offset:256 nt
PW 4,216,224
RQ 208,238,14336
v_mul_f32 v231, s71, v231
PW 4,184,228
v_exp_f32 v235, v235
PW 8,220,224
s_add_u32 m0, s61, 3968
RQ 200,238,13312
CV 201,234,235
PW 8,188,228
buffer_store_dwordx2 a[74:75], v242, s[52:55], s59 offen offset:1024 nt
v_mul_f32 v248, s71, v248
PW 12,224,224
RT 224,241,0
PW 12,192,228
s_waitcnt lgkmcnt(3)
RT 226,241,6144
PW 28,224,216
RT 228,241,12288
v_mul_f32 v229, s71, v229
PW 28,192,220
RT 230,241,18432
v_mul_f32 v250, s71, v250
PW 24,220,216
v_exp_f32 v232, v232
PW 24,188,220
buffer_load_dwordx4 v237, s[40:43], s58 offen offset:128 lds
PW 20,216,216
RQ 192,238,12288
v_mul_f32 v249, s71, v249
PW 20,184,220
v_mul_f32 v251, s71, v251
v_mul_f32 v230, s71, v230
PW 16,212,216
s_waitcnt lgkmcnt(2)
RQ 212,239,14336
v_mul_f32 v228, s71, v228
PW 16,180,220
s_add_u32 m0, s60, 0
v_exp_f32 v249, v249
PW 32,212,224
s_waitcnt lgkmcnt(2)
RQ 196,239,12288
PW 32,180,228
v_exp_f32 v248, v248
PW 36,216,224
RQ 204,239,13312
CV 200,232,233
PW 36,184,228
buffer_load_dwordx4 v237, s[44:47], s56 offen offset:0 lds
PW 40,220,224
RT 216,241,16
PW 40,188,228
v_exp_f32 v229, v229
PW 44,224,224
v_exp_f32 v250, v250
PW 44,192,228
s_waitcnt lgkmcnt(2)
v_exp_f32 v251, v251
QC 232,212,116
s_waitcnt lgkmcnt(1)
RT 220,241,12304
QK 232,208,112
s_add_u32 m0, s60, 896
v_exp_f32 v230, v230
QK 232,204,108
CV 204,248,249
CV 205,250,251
QK 232,200,104
buffer_load_dwordx4 v237, s[44:47], s56 offen offset:128 lds
QK 232,196,100
QK 232,192,96
v_exp_f32 v231, v231
QC 248,192,120
CV 209,230,231
QK 248,196,124
s_add_u32 m0, s61, 1920
v_exp_f32 v228, v228
QK 248,200,128
RT 224,240,1024
CV 208,228,229
QK 248,204,132
v_mul_f32 v233, s71, v233
v_mul_f32 v234, s71, v234
QK 248,208,136
v_exp_f32 v234, v234
QK 248,212,140
buffer_load_dwordx4 v237, s[40:43], s58 offen offset:3200 lds
QC 228,212,164
s_add_u32 m0, s60, 1792
v_mul_f32 v232, s71, v232
v_mul_f32 v235, s71, v235
QK 228,208,160
RT 218,241,6160
QK 228,204,156
v_exp_f32 v235, v235
QK 228,200,152
v_exp_f32 v233, v233
QK 228,196,148
v_exp_f32 v232, v232
QK 228,192,144
CV 198,232,233
CV 199,234,235
QC 232,192,168
RT 222,241,18448
QK 232,196,172
buffer_load_dwordx4 v237, s[44:47], s56 offen offset:256 lds
QK 232,200,176
RT 228,240,13312
v_mul_f32 v248, s71, v248
QK 232,204,180
v_mul_f32 v229, s71, v229
QK 232,208,184
RT 226,240,7168
QK 232,212,188
v_mul_f32 v251, s71, v251
v_smfmac_f32_16x16x64_bf16 a[232:235], a[240:243], v[188:195], v236
s_waitcnt lgkmcnt(2)
RQ 196,239,18432
v_mul_f32 v249, s71, v249
PW 60,224,216
RQ 200,238,19456
PW 60,192,220
RT 230,240,19456
v_mul_f32 v250, s71, v250
PW 56,220,216
v_exp_f32 v249, v249
PW 56,188,220
RQ 208,238,20480
L_c13_%=:
s_cbranch_execz L_ret_%=
v_mul_f32 v233, s71, v233
PW 52,216,216
RQ 192,238,18432
v_mul_f32 v231, s71, v231
PW 52,184,220
RQ 204,239,19456
v_mul_f32 v228, s71, v228
PW 48,212,216
RT 216,240,1040
PW 48,180,220
s_waitcnt lgkmcnt(4)
RT 218,240,7184
PW 64,212,224
RT 220,240,13328
PW 64,180,228
RT 222,240,19472
PW 68,216,224
s_add_u32 s77, s63, 184320
v_mul_f32 v230, s71, v230
PW 68,184,228
RQ 212,239,20480
PW 72,220,224
v_exp_f32 v231, v231
PW 72,188,228
s_add_u32 s56, s64, 6144
v_exp_f32 v230, v230
PW 76,224,224
CV 207,230,231
v_mul_f32 v234, s71, v234
PW 76,192,228
s_waitcnt lgkmcnt(1)
v_exp_f32 v248, v248
PW 92,224,216
v_exp_f32 v233, v233
PW 92,192,220
v_exp_f32 v250, v250
PW 88,220,216
v_exp_f32 v229, v229
PW 88,188,220
s_add_u32 m0, s60, 50048
v_exp_f32 v228, v228
PW 84,216,216
s_waitcnt vmcnt(19) lgkmcnt(0)
v_mul_f32 v232, s71, v232
v_mul_f32 v235, s71, v235
PW 84,184,220
)"
R"(
s_barrier
PW 80,212,216
L_s29_%=:
RT 230,241,31744
CV 206,228,229
PW 80,180,220
v_exp_f32 v235, v235
QC 228,212,116
v_exp_f32 v234, v234
QK 228,208,112
v_exp_f32 v232, v232
QK 228,204,108
v_exp_f32 v251, v251
QK 228,200,104
buffer_load_dwordx4 v237, s[48:51], s57 offen offset:128 lds
QK 228,196,100
CV 211,234,235
QK 228,192,96
RT 224,241,13312
CV 210,232,233
QC 232,192,120
CV 202,248,249
QK 232,196,124
RT 226,241,19456
QK 232,200,128
RT 222,241,31760
CV 203,250,251
QK 232,204,132
s_add_u32 m0, s60, 49152
RT 228,241,25600
v_mul_f32 v231, s71, v231
QK 232,208,136
v_mul_f32 v228, s71, v228
QK 232,212,140
buffer_load_dwordx4 v237, s[48:51], s57 offen offset:0 lds
QC 248,212,164
v_mul_f32 v229, s71, v229
v_mul_f32 v230, s71, v230
QK 248,208,160
v_exp_f32 v230, v230
QK 248,204,156
v_exp_f32 v229, v229
QK 248,200,152
v_exp_f32 v231, v231
QK 248,196,148
v_exp_f32 v228, v228
QK 248,192,144
CV 212,228,229
CV 213,230,231
QC 228,192,168
QK 228,196,172
v_mul_f32 v233, s71, v233
v_mul_f32 v235, s71, v235
QK 228,200,176
RT 218,241,19472
QK 228,204,180
buffer_store_dwordx2 a[76:77], v242, s[52:55], s59 offen offset:1792 nt
v_mul_f32 v248, s71, v248
QK 228,208,184
RT 216,241,13328
v_mul_f32 v234, s71, v234
QK 228,212,188
v_exp_f32 v233, v233
v_smfmac_f32_16x16x64_bf16 a[232:235], a[236:239], v[196:203], v236
s_waitcnt lgkmcnt(2)
RT 220,241,25616
v_mul_f32 v232, s71, v232
PW 96,180,224
v_exp_f32 v248, v248
PW 96,196,228
v_exp_f32 v234, v234
PW 100,184,224
RQ 212,239,26624
v_mul_f32 v228, s71, v228
PW 100,200,228
v_exp_f32 v232, v232
PW 104,188,224
buffer_store_dwordx2 a[78:79], v242, s[52:55], s59 offen offset:2560 nt
RQ 196,239,24576
PW 104,204,228
RQ 200,238,25600
v_mul_f32 v250, s71, v250
PW 108,192,224
RT 226,240,20480
PW 108,208,228
s_waitcnt lgkmcnt(4)
RT 230,240,32768
v_mul_f32 v229, s71, v229
PW 112,192,216
RT 224,240,14336
v_mul_f32 v231, s71, v231
PW 112,208,220
s_add_u32 s57, s64, 0
RT 228,240,26624
v_mul_f32 v249, s71, v249
v_mfma_f32_16x16x32_bf16 a[252:255], v[188:191], a[216:219], a[252:255]
v_exp_f32 v249, v249
v_mfma_f32_16x16x32_bf16 a[252:255], v[204:207], a[220:223], a[252:255]
s_add_u32 m0, s60, 61440
RQ 204,239,25600
v_mul_f32 v230, s71, v230
v_mfma_f32_16x16x32_bf16 a[248:251], v[184:187], a[216:219], a[248:251]
v_exp_f32 v228, v228
v_mfma_f32_16x16x32_bf16 a[248:251], v[200:203], a[220:223], a[248:251]
v_exp_f32 v235, v235
v_mfma_f32_16x16x32_bf16 a[244:247], v[180:183], a[216:219], a[244:247]
s_waitcnt lgkmcnt(2)
buffer_load_dwordx4 v237, s[48:51], s77 offen offset:0 lds
v_mfma_f32_16x16x32_bf16 a[244:247], v[196:199], a[220:223], a[244:247]
s_add_u32 s59, s68, 12288
v_exp_f32 v231, v231
PW 116,180,224
s_waitcnt lgkmcnt(1)
s_add_u32 s58, s70, 18432
RT 216,240,14352
PW 116,196,228
RQ 208,238,26624
CV 217,234,235
PW 120,184,224
v_mul_f32 v251, s71, v251
PW 120,200,228
v_exp_f32 v229, v229
PW 124,188,224
s_add_u32 m0, s61, 5888
RQ 192,238,24576
PW 124,204,228
v_exp_f32 v251, v251
PW 128,192,224
CV 216,232,233
CV 220,248,249
PW 128,208,228
v_exp_f32 v230, v230
QC 232,212,116
s_waitcnt lgkmcnt(1)
buffer_load_dwordx4 v237, s[40:43], s58 offen offset:256 lds
QK 232,208,112
RT 220,240,26640
QK 232,204,108
v_exp_f32 v250, v250
QK 232,200,104
s_add_u32 m0, s60, 12288
RT 230,241,32768
CV 224,228,229
QK 232,196,100
s_waitcnt lgkmcnt(2)
RT 218,240,20496
CV 221,250,251
QK 232,192,96
RT 224,241,14336
CV 225,230,231
QC 248,192,120
s_waitcnt vmcnt(27)
QK 248,196,124
QK 248,200,128
buffer_load_dwordx4 v237, s[44:47], s56 offen offset:0 lds
QK 248,204,132
s_add_u32 m0, s60, 13184
RT 228,241,26624
v_mul_f32 v234, s71, v234
QK 248,208,136
v_mul_f32 v235, s71, v235
QK 248,212,140
v_mul_f32 v232, s71, v232
v_mul_f32 v233, s71, v233
QC 228,212,164
v_exp_f32 v232, v232
QK 228,208,160
v_exp_f32 v235, v235
QK 228,204,156
v_exp_f32 v234, v234
QK 228,200,152
v_exp_f32 v233, v233
QK 228,196,148
RT 222,240,32784
v_mul_f32 v251, s71, v251
QK 228,192,144
CV 214,232,233
CV 215,234,235
QC 232,192,168
buffer_load_dwordx4 v237, s[44:47], s56 offen offset:128 lds
QK 232,196,172
RQ 76,245,14336
QK 232,200,176
QK 232,204,180
s_add_u32 m0, s61, 3840
RQ 72,244,14336
v_mul_f32 v248, s71, v248
QK 232,208,184
QK 232,212,188
RQ 204,239,31744
v_mul_f32 v249, s71, v249
v_smfmac_f32_16x16x64_bf16 a[232:235], a[240:243], v[204:211], v236
s_waitcnt lgkmcnt(3)
RT 226,241,20480
PW 144,192,216
RQ 196,239,30720
v_mul_f32 v230, s71, v230
PW 144,208,220
RQ 208,238,32768
v_mul_f32 v229, s71, v229
PW 140,188,216
buffer_load_dwordx4 v237, s[40:43], s58 offen offset:3328 lds
PW 140,204,220
s_add_u32 m0, s60, 14080
v_exp_f32 v229, v229
PW 136,184,216
RQ 192,238,30720
PW 136,200,220
v_exp_f32 v230, v230
PW 132,180,216
RT 218,241,20496
PW 132,196,220
s_waitcnt lgkmcnt(2)
RT 220,241,26640
v_mul_f32 v235, s71, v235
PW 148,180,224
RT 216,241,14352
v_mul_f32 v232, s71, v232
PW 148,196,228
RT 222,241,32784
PW 152,184,224
RQ 200,238,31744
v_mul_f32 v228, s71, v228
PW 152,200,228
RQ 212,239,32768
v_mul_f32 v231, s71, v231
PW 156,188,224
buffer_load_dwordx4 v237, s[44:47], s56 offen offset:256 lds
PW 156,204,228
v_mul_f32 v233, s71, v233
PW 160,192,224
v_exp_f32 v233, v233
PW 160,208,228
s_waitcnt lgkmcnt(2)
v_exp_f32 v228, v228
PW 176,192,216
v_exp_f32 v235, v235
PW 176,208,220
v_exp_f32 v251, v251
PW 172,188,216
v_exp_f32 v231, v231
PW 172,204,220
s_add_u32 m0, s60, 62336
v_mul_f32 v250, s71, v250
CV 222,228,229
PW 168,184,216
s_waitcnt vmcnt(19) lgkmcnt(0)
CV 223,230,231
v_mul_f32 v234, s71, v234
PW 168,200,220
s_barrier
PW 164,180,216
L_s30_%=:
buffer_load_dwordx4 v237, s[48:51], s77 offen offset:128 lds
PW 164,196,220
v_exp_f32 v248, v248
QC 228,212,116
v_exp_f32 v232, v232
QK 228,208,112
v_exp_f32 v249, v249
QK 228,204,108
CV 218,248,249
QK 228,200,104
v_exp_f32 v234, v234
QK 228,196,100
RT 228,240,36864
CV 226,232,233
QK 228,192,96
s_add_u32 m0, s60, 63232
CV 227,234,235
QC 232,192,120
buffer_load_dwordx4 v237, s[48:51], s77 offen offset:256 lds
QK 232,196,124
v_exp_f32 v250, v250
QK 232,200,128
CV 219,250,251
QK 232,204,132
v_mul_f32 v229, s71, v229
v_mul_f32 v231, s71, v231
QK 232,208,136
RT 230,240,43008
v_mul_f32 v228, s71, v228
QK 232,212,140
s_add_u32 m0, s60, 75520
RT 226,240,30720
v_mul_f32 v230, s71, v230
QC 248,212,164
v_exp_f32 v231, v231
QK 248,208,160
v_exp_f32 v229, v229
QK 248,204,156
v_exp_f32 v230, v230
QK 248,200,152
v_exp_f32 v228, v228
QK 248,196,148
buffer_store_dwordx2 a[80:81], v242, s[52:55], s59 offen offset:256 nt
CV 180,228,229
QK 248,192,144
RT 224,240,24576
CV 181,230,231
QC 228,192,168
RT 216,240,24592
v_mul_f32 v232, s71, v232
QK 228,196,172
buffer_load_dwordx4 v237, s[48:51], s57 offen offset:256 lds
QK 228,200,176
v_exp_f32 v232, v232
QK 228,204,180
v_mul_f32 v233, s71, v233
QK 228,208,184
RT 218,240,30736
QK 228,212,188
RQ 196,239,36864
v_mul_f32 v248, s71, v248
v_smfmac_f32_16x16x64_bf16 a[232:235], a[236:239], v[212:219], v236
s_waitcnt lgkmcnt(2)
RQ 212,239,38912
v_mul_f32 v250, s71, v250
PW 0,196,224
RT 220,240,36880
v_mul_f32 v249, s71, v249
PW 0,212,228
RT 222,240,43024
PW 4,200,224
v_exp_f32 v250, v250
PW 4,216,228
v_exp_f32 v249, v249
PW 8,204,224
buffer_store_dwordx2 a[82:83], v242, s[52:55], s59 offen offset:1024 nt
RQ 200,238,37888
PW 8,220,228
v_mul_f32 v251, s71, v251
v_mul_f32 v230, s71, v230
PW 12,208,224
RT 224,241,24576
v_mul_f32 v234, s71, v234
PW 12,224,228
s_waitcnt lgkmcnt(2)
RT 226,241,30720
PW 28,208,216
RT 230,241,43008
PW 28,224,220
RT 228,241,36864
v_mul_f32 v235, s71, v235
PW 24,204,216
PW 24,220,220
v_exp_f32 v251, v251
PW 20,200,216
v_exp_f32 v233, v233
PW 20,216,220
v_exp_f32 v235, v235
PW 16,196,216
buffer_store_dwordx2 a[84:85], v242, s[52:55], s59 offen offset:1792 nt
RQ 204,239,37888
PW 16,212,220
s_waitcnt lgkmcnt(1)
RQ 208,238,38912
v_mul_f32 v229, s71, v229
PW 32,196,224
buffer_store_dwordx2 a[86:87], v242, s[52:55], s59 offen offset:2560 nt
CV 184,232,233
PW 32,212,228
v_exp_f32 v234, v234
PW 36,200,224
RQ 192,238,36864
PW 36,216,228
s_add_u32 m0, s60, 24576
v_exp_f32 v248, v248
PW 40,204,224
s_add_u32 s56, s64, 18432
v_mul_f32 v228, s71, v228
PW 40,220,228
s_add_u32 s59, s68, 18432
CV 185,234,235
CV 188,248,249
PW 44,208,224
v_exp_f32 v228, v228
PW 44,224,228
RT 222,241,43024
v_mul_f32 v231, s71, v231
QC 232,212,116
s_waitcnt lgkmcnt(1)
v_exp_f32 v229, v229
QK 232,208,112
buffer_load_dwordx4 v237, s[44:47], s56 offen offset:0 lds
QK 232,204,108
RT 218,241,30736
CV 192,228,229
QK 232,200,104
RT 224,240,25600
CV 189,250,251
QK 232,196,100
v_exp_f32 v231, v231
QK 232,192,96
v_exp_f32 v230, v230
QC 248,192,120
s_waitcnt vmcnt(19)
RT 220,241,36880
QK 248,196,124
CV 193,230,231
)"
R"(
QK 248,200,128
buffer_store_dwordx2 a[88:89], v242, s[52:55], s59 offen offset:256 nt
RT 216,241,24592
QK 248,204,132
v_mul_f32 v233, s71, v233
v_mul_f32 v234, s71, v234
QK 248,208,136
s_add_u32 m0, s60, 25472
v_mul_f32 v235, s71, v235
QK 248,212,140
v_exp_f32 v233, v233
QC 228,212,164
v_mul_f32 v232, s71, v232
QK 228,208,160
v_exp_f32 v232, v232
QK 228,204,156
v_exp_f32 v235, v235
QK 228,200,152
v_exp_f32 v234, v234
QK 228,196,148
CV 182,232,233
CV 183,234,235
QK 228,192,144
buffer_load_dwordx4 v237, s[44:47], s56 offen offset:128 lds
QC 232,192,168
RT 230,240,44032
QK 232,196,172
RQ 80,244,4096
QK 232,200,176
QK 232,204,180
RQ 196,239,43008
v_mul_f32 v250, s71, v250
QK 232,208,184
RQ 84,245,4096
v_mul_f32 v228, s71, v228
QK 232,212,188
RQ 192,238,43008
v_mul_f32 v229, s71, v229
v_smfmac_f32_16x16x64_bf16 a[232:235], a[240:243], v[220:227], v236
s_waitcnt lgkmcnt(4)
RT 226,240,31744
PW 60,208,216
RQ 200,238,44032
v_mul_f32 v231, s71, v231
PW 60,224,220
RT 228,240,37888
v_mul_f32 v230, s71, v230
PW 56,204,216
buffer_store_dwordx2 a[90:91], v242, s[52:55], s59 offen offset:1024 nt
RQ 212,239,45056
PW 56,220,220
s_add_u32 m0, s60, 26368
RQ 208,238,45056
v_mul_f32 v232, s71, v232
PW 52,200,216
v_exp_f32 v250, v250
PW 52,216,220
RQ 204,239,44032
v_mul_f32 v234, s71, v234
PW 48,196,216
RT 218,240,31760
v_mul_f32 v233, s71, v233
PW 48,212,220
s_waitcnt lgkmcnt(4)
RT 222,240,44048
v_mul_f32 v235, s71, v235
PW 64,196,224
RT 216,240,25616
v_mul_f32 v248, s71, v248
PW 64,212,228
RT 220,240,37904
v_mul_f32 v249, s71, v249
PW 68,200,224
s_add_u32 s77, s64, 12288
v_exp_f32 v229, v229
PW 68,216,228
v_exp_f32 v228, v228
PW 72,204,224
buffer_load_dwordx4 v237, s[44:47], s56 offen offset:256 lds
PW 72,220,228
s_add_u32 m0, s60, 73728
v_exp_f32 v232, v232
PW 76,208,224
v_exp_f32 v231, v231
PW 76,224,228
s_waitcnt lgkmcnt(0)
v_exp_f32 v230, v230
PW 92,208,216
v_exp_f32 v235, v235
PW 92,224,220
CV 190,228,229
CV 191,230,231
PW 88,204,216
v_exp_f32 v249, v249
PW 88,220,220
v_exp_f32 v233, v233
PW 84,200,216
s_waitcnt vmcnt(21)
v_exp_f32 v248, v248
PW 84,216,220
s_barrier
PW 80,196,216
L_s31_%=:
buffer_load_dwordx4 v237, s[48:51], s57 offen offset:0 lds
PW 80,212,220
CV 186,248,249
QC 228,212,116
v_exp_f32 v234, v234
QK 228,208,112
RT 226,241,44032
CV 194,232,233
QK 228,204,108
s_add_u32 m0, s60, 74624
RT 224,241,37888
CV 195,234,235
QK 228,200,104
RT 220,241,1040
QK 228,196,100
RT 228,241,1024
QK 228,192,96
RT 230,241,7168
v_mul_f32 v251, s71, v251
QC 232,192,120
buffer_load_dwordx4 v237, s[48:51], s57 offen offset:128 lds
QK 232,196,124
v_exp_f32 v251, v251
QK 232,200,128
RT 222,241,7184
CV 187,250,251
QK 232,204,132
RT 216,241,37904
v_mul_f32 v230, s71, v230
QK 232,208,136
s_add_u32 m0, s60, 86016
RT 218,241,44048
v_mul_f32 v229, s71, v229
QK 232,212,140
v_mul_f32 v231, s71, v231
v_smfmac_f32_16x16x64_bf16 a[232:235], a[236:239], v[180:187], v236
s_waitcnt lgkmcnt(3)
buffer_store_dwordx2 a[92:93], v242, s[52:55], s59 offen offset:1792 nt
PW 96,212,224
v_exp_f32 v231, v231
PW 96,180,228
buffer_load_dwordx4 v237, s[48:51], s77 offen offset:0 lds
PW 100,216,224
v_mul_f32 v228, s71, v228
PW 100,184,228
buffer_store_dwordx2 a[94:95], v242, s[52:55], s59 offen offset:2560 nt
v_mul_f32 v235, s71, v235
PW 104,220,224
v_exp_f32 v230, v230
PW 104,188,228
v_exp_f32 v229, v229
PW 108,224,224
PW 108,192,228
s_waitcnt lgkmcnt(0)
RT 228,240,2048
CV 197,230,231
PW 112,224,216
L_c14_%=:
s_cbranch_execz L_ret_%=
v_mul_f32 v234, s71, v234
PW 112,192,220
v_exp_f32 v228, v228
v_mfma_f32_16x16x32_bf16 a[252:255], v[220:223], a[216:219], a[252:255]
s_add_u32 s87, s64, 147456
CV 196,228,229
v_mul_f32 v233, s71, v233
v_mfma_f32_16x16x32_bf16 a[252:255], v[188:191], a[220:223], a[252:255]
v_exp_f32 v234, v234
QC 228,212,164
v_exp_f32 v235, v235
QK 228,208,160
v_exp_f32 v233, v233
QK 228,204,156
RT 224,240,38912
QK 228,200,152
v_mul_f32 v232, s71, v232
QK 228,196,148
v_exp_f32 v232, v232
QK 228,192,144
CV 200,232,233
CV 201,234,235
QC 232,192,168
RT 230,240,8192
QK 232,196,172
s_add_u32 m0, s60, 36864
QK 232,200,176
RT 226,240,45056
QK 232,204,180
RQ 200,238,1024
v_mul_f32 v231, s71, v231
QK 232,208,184
s_add_u32 s56, s64, 30720
RQ 208,238,2048
v_mul_f32 v230, s71, v230
QK 232,212,188
RQ 204,239,1024
v_mul_f32 v229, s71, v229
v_mfma_f32_16x16x32_bf16 a[248:251], v[216:219], a[216:219], a[248:251]
v_exp_f32 v229, v229
v_mfma_f32_16x16x32_bf16 a[248:251], v[184:187], a[220:223], a[248:251]
v_exp_f32 v230, v230
v_mfma_f32_16x16x32_bf16 a[244:247], v[212:215], a[216:219], a[244:247]
RT 216,240,38928
v_mul_f32 v228, s71, v228
v_mfma_f32_16x16x32_bf16 a[244:247], v[180:183], a[220:223], a[244:247]
s_waitcnt lgkmcnt(3)
RQ 212,239,2048
v_mul_f32 v235, s71, v235
PW 116,212,224
RQ 196,239,0
v_mul_f32 v234, s71, v234
PW 116,180,228
RQ 192,238,0
v_mul_f32 v232, s71, v232
PW 120,216,224
v_exp_f32 v228, v228
PW 120,184,228
v_exp_f32 v234, v234
PW 124,220,224
v_exp_f32 v235, v235
PW 124,188,228
buffer_load_dwordx4 v237, s[44:47], s56 offen offset:0 lds
PW 128,224,224
s_add_u32 m0, s60, 37760
v_exp_f32 v231, v231
PW 128,192,228
s_waitcnt lgkmcnt(0)
CV 204,228,229
CV 205,230,231
QC 228,212,116
v_exp_f32 v232, v232
QK 228,208,112
QK 228,204,108
RT 218,240,45072
v_mul_f32 v233, s71, v233
QK 228,200,104
RT 222,240,8208
CV 209,234,235
QK 228,196,100
v_exp_f32 v233, v233
QK 228,192,96
RT 220,240,2064
CV 208,232,233
QC 232,192,120
s_waitcnt vmcnt(19)
QK 232,196,124
buffer_load_dwordx4 v237, s[44:47], s56 offen offset:128 lds
QK 232,200,128
RT 226,241,45056
QK 232,204,132
RT 230,241,8192
QK 232,208,136
RT 228,241,2048
v_mul_f32 v231, s71, v231
QK 232,212,140
RT 224,241,38912
v_smfmac_f32_16x16x64_bf16 a[232:235], a[240:243], v[188:195], v236
s_waitcnt lgkmcnt(4)
RQ 92,245,6144
PW 144,224,216
RQ 88,244,6144
v_mul_f32 v228, s71, v228
PW 144,192,220
v_exp_f32 v231, v231
PW 140,220,216
s_add_u32 m0, s60, 38656
v_mul_f32 v230, s71, v230
v_mul_f32 v232, s71, v232
PW 140,188,220
buffer_load_dwordx4 v237, s[44:47], s56 offen offset:256 lds
PW 136,216,216
v_mul_f32 v229, s71, v229
v_mul_f32 v235, s71, v235
PW 136,184,220
v_exp_f32 v228, v228
PW 132,212,216
v_exp_f32 v230, v230
PW 132,180,220
s_waitcnt lgkmcnt(0)
CV 199,230,231
PW 148,212,224
v_exp_f32 v229, v229
PW 148,180,228
v_exp_f32 v232, v232
PW 152,216,224
s_add_u32 m0, s60, 86912
v_exp_f32 v235, v235
PW 152,184,228
CV 198,228,229
v_mul_f32 v233, s71, v233
QC 228,212,164
RT 220,241,2064
QK 228,208,160
v_exp_f32 v233, v233
QK 228,204,156
v_mul_f32 v234, s71, v234
QK 228,200,152
v_exp_f32 v234, v234
QK 228,196,148
RT 218,241,45072
CV 202,232,233
QK 228,192,144
RT 222,241,8208
CV 203,234,235
QC 232,192,168
RQ 192,238,6144
QK 232,196,172
RQ 196,239,6144
QK 232,200,176
RQ 200,238,7168
QK 232,204,180
RT 216,241,38928
v_mul_f32 v229, s71, v229
QK 232,208,184
QK 232,212,188
RQ 208,238,8192
v_mul_f32 v228, s71, v228
v_smfmac_f32_16x16x64_bf16 a[232:235], a[236:239], v[196:203], v236
s_add_u32 s56, s64, 43008
RQ 204,239,7168
v_mul_f32 v231, s71, v231
PW 156,220,224
RQ 212,239,8192
PW 156,188,228
s_add_u32 s57, s64, 24576
v_exp_f32 v229, v229
PW 160,224,224
v_exp_f32 v228, v228
PW 160,192,228
s_waitcnt lgkmcnt(3)
v_mul_f32 v230, s71, v230
PW 176,224,216
v_exp_f32 v231, v231
PW 176,192,220
v_exp_f32 v230, v230
PW 172,220,216
v_mul_f32 v232, s71, v232
v_mul_f32 v234, s71, v234
PW 172,188,220
v_exp_f32 v234, v234
PW 168,216,216
s_waitcnt vmcnt(19) lgkmcnt(0)
CV 206,228,229
v_mul_f32 v233, s71, v233
PW 168,184,220
s_barrier
PW 164,212,216
L_s32_%=:
buffer_load_dwordx4 v237, s[48:51], s77 offen offset:128 lds
PW 164,180,220
RT 226,240,6144
CV 207,230,231
QC 228,212,20
RT 224,240,0
v_mul_f32 v235, s71, v235
QK 228,208,16
v_exp_f32 v233, v233
QK 228,204,12
s_add_u32 m0, s60, 87808
v_exp_f32 v232, v232
QK 228,200,8
v_exp_f32 v235, v235
QK 228,196,4
RT 228,240,12288
CV 211,234,235
QK 228,192,0
RT 230,240,18432
CV 210,232,233
QC 232,192,24
buffer_load_dwordx4 v237, s[48:51], s77 offen offset:256 lds
QK 232,196,28
RT 222,240,18448
QK 232,200,32
RT 220,240,12304
QK 232,204,36
RT 216,240,16
v_mul_f32 v229, s71, v229
QK 232,208,40
v_exp_f32 v229, v229
QK 232,212,44
v_mul_f32 v228, s71, v228
v_smfmac_f32_16x16x64_bf16 a[232:235], a[240:243], v[204:211], v236
s_waitcnt lgkmcnt(3)
v_exp_f32 v228, v228
PW 0,180,224
RT 218,240,6160
v_mul_f32 v230, s71, v230
PW 0,196,228
s_add_u32 m0, s60, 50944
v_exp_f32 v230, v230
PW 4,184,224
buffer_load_dwordx4 v237, s[48:51], s57 offen offset:256 lds
PW 4,200,228
v_mul_f32 v231, s71, v231
CV 212,228,229
PW 8,188,224
v_accvgpr_read_b32 v252, a232
)"
R"(
v_accvgpr_read_b32 v248, a233
PW 8,204,228
v_exp_f32 v231, v231
PW 12,192,224
v_mul_f32 v232, s71, v232
PW 12,208,228
s_waitcnt lgkmcnt(0)
CV 213,230,231
v_mul_f32 v234, s71, v234
PW 28,192,216
RT 228,241,12288
PW 28,208,220
v_exp_f32 v232, v232
PW 24,188,216
v_mul_f32 v233, s71, v233
v_mul_f32 v235, s71, v235
PW 24,204,220
RT 230,241,18432
v_add_f32 v252, v252, v248
QC 228,212,68
v_exp_f32 v233, v233
QK 228,208,64
v_exp_f32 v235, v235
QK 228,204,60
v_rcp_f32 v252, v252
QK 228,200,56
s_mov_b32 m0, s80
v_exp_f32 v234, v234
QK 228,196,52
RT 224,241,0
CV 216,232,233
QK 228,192,48
RT 226,241,6144
CV 217,234,235
QC 232,192,72
ds_write_addtid_b32 v252 offset:65280
QK 232,196,76
s_add_u32 s77, s64, 36864
ds_read_b128 v[248:251], v254 offset:0
QK 232,200,80
RQ 196,239,12288
v_accvgpr_write_b32 a232, 0
QK 232,204,84
s_add_u32 m0, s60, 118784
RQ 200,238,13312
v_mul_f32 v230, s71, v230
QK 232,208,88
buffer_load_dwordx4 v237, s[48:51], s87 offen offset:0 lds
QK 232,212,92
RQ 192,238,12288
v_mul_f32 v231, s71, v231
PW 20,184,216
RQ 212,239,14336
v_mul_f32 v228, s71, v228
PW 20,200,220
v_exp_f32 v231, v231
PW 16,180,216
v_exp_f32 v228, v228
PW 16,196,220
s_waitcnt lgkmcnt(2)
RQ 208,238,14336
v_mul_f32 v229, s71, v229
PW 32,180,224
RT 218,241,6160
v_mul_f32 v233, s71, v233
PW 32,196,228
v_exp_f32 v229, v229
PW 36,184,224
RQ 204,239,13312
v_mul_f32 v235, s71, v235
PW 36,200,228
s_add_u32 m0, s60, 0
RT 220,241,12304
v_mul_f32 v232, s71, v232
PW 40,188,224
buffer_load_dwordx4 v237, s[44:47], s56 offen offset:0 lds
PW 40,204,228
v_exp_f32 v230, v230
PW 44,192,224
s_waitcnt lgkmcnt(2)
RT 226,240,7168
CV 221,230,231
PW 44,208,228
s_add_u32 m0, s60, 119680
CV 220,228,229
v_mul_f32 v234, s71, v234
QC 228,212,20
v_exp_f32 v235, v235
QK 228,208,16
s_waitcnt lgkmcnt(1)
v_exp_f32 v233, v233
QK 228,204,12
v_exp_f32 v234, v234
QK 228,200,8
v_exp_f32 v232, v232
QK 228,196,4
RT 216,241,16
CV 225,234,235
QK 228,192,0
RT 222,241,18448
CV 224,232,233
QC 232,192,24
buffer_load_dwordx4 v237, s[48:51], s87 offen offset:128 lds
QK 232,196,28
RT 224,240,1024
QK 232,200,32
RT 228,240,13312
QK 232,204,36
v_mul_f32 v228, s71, v228
QK 232,208,40
s_add_u32 m0, s60, 896
RT 230,240,19456
QK 232,212,44
s_waitcnt lgkmcnt(3)
v_exp_f32 v228, v228
PW 60,192,216
v_mul_f32 v229, s71, v229
v_mul_f32 v231, s71, v231
PW 60,208,220
v_exp_f32 v229, v229
PW 56,188,216
buffer_load_dwordx4 v237, s[44:47], s56 offen offset:128 lds
PW 56,204,220
v_accvgpr_write_b32 a233, 0
v_mul_f32 v230, s71, v230
v_exp_f32 v230, v230
v_mul_f32 v235, s71, v235
v_pk_mul_f32 v[0:1], v[0:1], v[248:249]
v_pk_mul_f32 v[2:3], v[2:3], v[250:251]
v_pk_mul_f32 v[16:17], v[16:17], v[248:249]
v_pk_mul_f32 v[18:19], v[18:19], v[250:251]
CV 0,0,16
PW 52,184,216
v_exp_f32 v231, v231
PW 52,200,220
CV 215,230,231
v_mul_f32 v234, s71, v234
PW 48,180,216
CV 214,228,229
CV 16,2,18
PW 48,196,220
s_waitcnt lgkmcnt(0)
v_mul_f32 v232, s71, v232
v_mul_f32 v233, s71, v233
PW 64,180,224
v_exp_f32 v233, v233
PW 64,196,228
s_add_u32 m0, s60, 1792
v_exp_f32 v232, v232
PW 68,184,224
buffer_load_dwordx4 v237, s[44:47], s56 offen offset:256 lds
PW 68,200,228
RT 218,240,7184
CV 18,3,19
QC 228,212,68
CV 2,1,17
v_exp_f32 v234, v234
v_exp_f32 v235, v235
v_pk_mul_f32 v[32:33], v[32:33], v[248:249]
v_pk_mul_f32 v[34:35], v[34:35], v[250:251]
v_pk_mul_f32 v[48:49], v[48:49], v[248:249]
v_pk_mul_f32 v[50:51], v[50:51], v[250:251]
CV 19,35,51
QK 228,208,64
ds_read_b128 v[248:251], v254 offset:64
QK 228,204,60
RT 222,240,19472
CV 3,33,49
QK 228,200,56
s_add_u32 m0, s60, 120576
RT 220,240,13328
CV 219,234,235
QK 228,196,52
v_cmp_lt_u32 vcc, s75, v252
CV 17,34,50
QK 228,192,48
RT 216,240,1040
CV 218,232,233
QC 232,192,72
buffer_load_dwordx4 v237, s[48:51], s87 offen offset:256 lds
QK 232,196,76
RQ 196,239,18432
CV 1,32,48
QK 232,200,80
s_add_u32 s56, s64, 165888
RQ 192,238,18432
QK 232,204,84
s_waitcnt lgkmcnt(3)
RQ 204,239,19456
ds_write_b128 v246, v[0:3] offset:0
v_mul_f32 v229, s71, v229
v_pk_mul_f32 v[4:5], v[4:5], v[248:249]
v_pk_mul_f32 v[6:7], v[6:7], v[250:251]
v_pk_mul_f32 v[20:21], v[20:21], v[248:249]
v_pk_mul_f32 v[22:23], v[22:23], v[250:251]
v_pk_mul_f32 v[36:37], v[36:37], v[248:249]
v_pk_mul_f32 v[38:39], v[38:39], v[250:251]
v_pk_mul_f32 v[52:53], v[52:53], v[248:249]
v_pk_mul_f32 v[54:55], v[54:55], v[250:251]
v_mul_f32 v230, s71, v230
QK 232,208,88
ds_read_b128 v[248:251], v254 offset:128
CV 4,4,20
QK 232,212,92
RQ 208,238,20480
CV 20,6,22
PW 72,188,224
RQ 200,238,19456
v_exp_f32 v230, v230
PW 72,204,228
RQ 212,239,20480
v_exp_f32 v229, v229
PW 76,192,224
RT 100,247,320
v_mul_f32 v228, s71, v228
PW 76,208,228
s_waitcnt lgkmcnt(4)
ds_write_b128 v246, v[16:19] offset:1280
CV 6,5,21
v_pk_mul_f32 v[8:9], v[8:9], v[248:249]
v_pk_mul_f32 v[10:11], v[10:11], v[250:251]
v_pk_mul_f32 v[24:25], v[24:25], v[248:249]
v_pk_mul_f32 v[26:27], v[26:27], v[250:251]
v_pk_mul_f32 v[40:41], v[40:41], v[248:249]
v_pk_mul_f32 v[42:43], v[42:43], v[250:251]
v_pk_mul_f32 v[56:57], v[56:57], v[248:249]
v_pk_mul_f32 v[58:59], v[58:59], v[250:251]
CV 22,7,23
PW 92,192,216
ds_read_b128 v[248:251], v254 offset:192
CV 5,36,52
v_mul_f32 v234, s71, v234
PW 92,208,220
s_add_u32 m0, s60, 50048
v_exp_f32 v228, v228
PW 88,188,216
RT 96,247,0
CV 8,8,24
PW 88,204,220
v_mul_f32 v231, s71, v231
v_mul_f32 v232, s71, v232
PW 84,184,216
s_waitcnt vmcnt(15) lgkmcnt(4)
v_mul_f32 v233, s71, v233
v_mul_f32 v235, s71, v235
v_exp_f32 v234, v234
PW 84,200,220
s_barrier
RT 98,247,1280
v_exp_f32 v231, v231
PW 80,180,216
L_s33_%=:
RT 230,241,19472
CV 7,37,53
CV 23,39,55
CV 24,10,26
PW 80,196,220
s_waitcnt lgkmcnt(2)
RT 226,241,19456
ds_write_b128 v246, v[4:7] offset:0
CV 222,228,229
CV 223,230,231
CV 26,11,27
v_pk_mul_f32 v[12:13], v[12:13], v[248:249]
v_pk_mul_f32 v[14:15], v[14:15], v[250:251]
v_pk_mul_f32 v[28:29], v[28:29], v[248:249]
v_pk_mul_f32 v[30:31], v[30:31], v[250:251]
v_pk_mul_f32 v[44:45], v[44:45], v[248:249]
v_pk_mul_f32 v[46:47], v[46:47], v[250:251]
v_pk_mul_f32 v[60:61], v[60:61], v[248:249]
v_pk_mul_f32 v[62:63], v[62:63], v[250:251]
CV 27,43,59
QC 228,212,20
buffer_load_dwordx4 v237, s[48:51], s57 offen offset:128 lds
RT 224,241,13312
CV 10,9,25
QK 228,208,16
RT 228,241,13328
v_exp_f32 v235, v235
QK 228,204,12
ds_read_b128 v[248:251], v254 offset:0
v_exp_f32 v233, v233
CV 12,12,28
QK 228,200,8
s_add_u32 m0, s60, 135168
v_exp_f32 v232, v232
QK 228,196,4
RT 216,240,14336
CV 226,232,233
QK 228,192,0
RT 222,240,20496
CV 227,234,235
QC 232,192,24
RT 218,240,20480
CV 21,38,54
QK 232,196,28
RT 102,247,1600
CV 9,40,56
QK 232,200,32
buffer_load_dwordx4 v237, s[44:47], s56 offen offset:0 lds
RT 104,247,0
QK 232,204,36
RT 108,247,320
v_mul_f32 v231, s71, v231
QK 232,208,40
s_waitcnt lgkmcnt(5)
RT 220,240,14352
ds_write_b128 v246, v[20:23] offset:1280
v_pk_mul_f32 v[64:65], v[64:65], v[248:249]
v_pk_mul_f32 v[66:67], v[66:67], v[250:251]
v_pk_mul_f32 v[80:81], v[80:81], v[248:249]
v_pk_mul_f32 v[82:83], v[82:83], v[250:251]
CV 11,41,57
QK 232,212,44
s_add_u32 s59, s69, 0
CV 25,42,58
CV 64,64,80
v_smfmac_f32_16x16x64_bf16 a[232:235], a[236:239], v[212:219], v236
s_add_u32 s87, s64, 159744
CV 28,14,30
v_exp_f32 v231, v231
PW 96,196,224
buffer_store_dwordx2 a[96:97], v242, s[52:55], s59 offen offset:0 nt
v_mul_f32 v228, s71, v228
v_mul_f32 v230, s71, v230
PW 100,200,224
s_add_u32 m0, s60, 49152
CV 14,13,29
CV 80,66,82
CV 66,65,81
v_mfma_f32_16x16x32_bf16 a[244:247], v[196:199], a[228:231], a[244:247]
v_mul_f32 v229, s71, v229
v_exp_f32 v228, v228
v_mfma_f32_16x16x32_bf16 a[248:251], v[200:203], a[228:231], a[248:251]
RT 106,247,1280
v_mul_f32 v232, s71, v232
v_mul_f32 v234, s71, v234
PW 104,204,224
buffer_load_dwordx4 v237, s[48:51], s57 offen offset:0 lds
RT 110,247,1600
CV 82,67,83
PW 108,208,224
CV 30,15,31
v_exp_f32 v230, v230
v_exp_f32 v234, v234
v_mfma_f32_16x16x32_bf16 a[252:255], v[204:207], a[228:231], a[252:255]
ds_write_b128 v246, v[24:27] offset:1280
v_accvgpr_read_b32 v65, a244
v_accvgpr_read_b32 v83, a247
PW 112,208,228
s_waitcnt lgkmcnt(2)
v_mul_f32 v235, s71, v235
v_exp_f32 v232, v232
v_accvgpr_read_b32 v81, a246
v_mul_f32 v83, v83, v251
PW 128,208,216
L_c15_%=:
s_cbranch_execz L_ret_%=
buffer_store_dwordx2 a[98:99], v242, s[52:55], s59 offen offset:768 nt
RT 226,241,20480
ds_write_b128 v246, v[8:11] offset:0
v_mul_f32 v233, s71, v233
v_accvgpr_read_b32 v67, a245
v_mul_f32 v65, v65, v248
v_mul_f32 v67, v67, v249
v_pk_mul_f32 v[96:97], v[96:97], v[248:249]
v_pk_mul_f32 v[98:99], v[98:99], v[250:251]
v_mul_f32 v81, v81, v250
PW 124,204,216
ds_read_b128 v[248:251], v254 offset:64
v_exp_f32 v229, v229
CV 13,44,60
PW 144,208,220
CV 180,228,229
CV 181,230,231
v_exp_f32 v233, v233
PW 140,204,220
RT 114,247,1280
CV 81,98,81
QC 228,212,68
s_add_u32 m0, s60, 136064
RT 228,241,14352
CV 29,46,62
QK 228,208,64
buffer_load_dwordx4 v237, s[44:47], s56 offen offset:128 lds
RT 112,247,0
QK 228,204,60
RT 118,247,1600
CV 184,232,233
QK 228,200,56
RT 230,241,20496
CV 15,45,61
QK 228,196,52
RT 116,247,320
v_exp_f32 v235, v235
QK 228,192,48
s_waitcnt lgkmcnt(4)
ds_write_b128 v246, v[12:15] offset:0
v_pk_mul_f32 v[70:71], v[70:71], v[250:251]
v_pk_mul_f32 v[84:85], v[84:85], v[248:249]
)"
R"(
v_pk_mul_f32 v[100:101], v[100:101], v[248:249]
v_pk_mul_f32 v[102:103], v[102:103], v[250:251]
CV 185,234,235
QC 232,192,72
CV 31,47,63
CV 67,97,67
QK 232,196,76
RT 224,241,14336
ds_write_b128 v246, v[28:31] offset:1280
v_pk_mul_f32 v[68:69], v[68:69], v[248:249]
v_pk_mul_f32 v[86:87], v[86:87], v[250:251]
CV 68,68,84
CV 84,70,86
CV 86,71,87
QK 232,200,80
CV 70,69,85
v_accvgpr_read_b32 v71, a249
v_accvgpr_read_b32 v85, a250
v_accvgpr_read_b32 v87, a251
v_mul_f32 v71, v71, v249
QK 232,204,84
RQ 200,238,25600
v_accvgpr_read_b32 v69, a248
v_mul_f32 v87, v87, v251
QK 232,208,88
RT 120,247,0
v_mul_f32 v229, s71, v229
v_mul_f32 v69, v69, v248
v_mul_f32 v85, v85, v250
QK 232,212,92
ds_read_b128 v[248:251], v254 offset:128
v_mul_f32 v230, s71, v230
PW 120,200,216
RQ 212,239,26624
CV 83,99,83
CV 87,103,87
PW 116,196,216
s_add_u32 m0, s60, 61440
RQ 208,238,26624
v_mul_f32 v228, s71, v228
PW 136,200,220
RT 126,247,1600
v_mul_f32 v231, s71, v231
PW 132,196,220
s_waitcnt lgkmcnt(4)
RT 122,247,1280
v_mul_f32 v232, s71, v232
PW 148,196,224
RQ 196,239,24576
v_exp_f32 v230, v230
v_mul_f32 v234, s71, v234
v_exp_f32 v234, v234
PW 152,200,224
RQ 204,239,25600
CV 65,96,65
PW 164,196,228
s_waitcnt lgkmcnt(4)
ds_write_b128 v246, v[80:83] offset:1280
v_exp_f32 v228, v228
v_pk_mul_f32 v[72:73], v[72:73], v[248:249]
v_pk_mul_f32 v[74:75], v[74:75], v[250:251]
v_pk_mul_f32 v[88:89], v[88:89], v[248:249]
v_pk_mul_f32 v[90:91], v[90:91], v[250:251]
v_pk_mul_f32 v[106:107], v[106:107], v[250:251]
v_mul_f32 v235, s71, v235
PW 168,200,228
RQ 192,238,24576
v_exp_f32 v229, v229
v_exp_f32 v231, v231
v_pk_mul_f32 v[104:105], v[104:105], v[248:249]
v_exp_f32 v235, v235
PW 156,204,224
RT 222,240,30736
CV 189,230,231
PW 160,208,224
CV 72,72,88
CV 88,74,90
CV 74,73,89
CV 90,75,91
PW 172,204,228
CV 188,228,229
v_accvgpr_read_b32 v75, a253
v_accvgpr_read_b32 v91, a255
PW 176,208,228
CV 85,102,85
v_accvgpr_read_b32 v89, a254
QC 228,212,20
buffer_load_dwordx4 v237, s[48:51], s77 offen offset:0 lds
RT 216,240,24576
v_accvgpr_read_b32 v73, a252
v_mul_f32 v75, v75, v249
QK 228,208,16
s_waitcnt lgkmcnt(1)
RT 124,247,320
v_mul_f32 v233, s71, v233
v_exp_f32 v232, v232
v_mul_f32 v73, v73, v248
QK 228,204,12
RT 134,247,1600
v_exp_f32 v233, v233
QK 228,200,8
RT 224,241,24576
v_mul_f32 v89, v89, v250
v_mul_f32 v91, v91, v251
QK 228,196,4
ds_read_b128 v[248:251], v254 offset:192
CV 192,232,233
QK 228,192,0
s_add_u32 m0, s60, 136960
RT 218,240,30720
CV 193,234,235
QC 232,192,24
RT 228,241,24592
CV 71,101,71
QK 232,196,28
RT 220,240,24592
CV 73,104,73
QK 232,200,32
buffer_load_dwordx4 v237, s[44:47], s56 offen offset:256 lds
RT 130,247,1280
QK 232,204,36
s_add_u32 m0, s60, 12288
v_mul_f32 v229, s71, v229
v_mul_f32 v231, s71, v231
QK 232,208,40
s_add_u32 s56, s64, 55296
v_exp_f32 v231, v231
QK 232,212,44
s_waitcnt lgkmcnt(3)
RT 230,241,30736
ds_write_b128 v246, v[64:67] offset:0
v_pk_mul_f32 v[78:79], v[78:79], v[250:251]
v_pk_mul_f32 v[92:93], v[92:93], v[248:249]
v_pk_mul_f32 v[94:95], v[94:95], v[250:251]
v_pk_mul_f32 v[108:109], v[108:109], v[248:249]
v_pk_mul_f32 v[110:111], v[110:111], v[250:251]
v_pk_mul_f32 v[114:115], v[114:115], v[250:251]
CV 89,106,89
v_smfmac_f32_16x16x64_bf16 a[232:235], a[240:243], v[220:227], v236
RT 226,241,30720
ds_write_b128 v246, v[84:87] offset:1280
CV 75,105,75
v_pk_mul_f32 v[76:77], v[76:77], v[248:249]
v_pk_mul_f32 v[112:113], v[112:113], v[248:249]
v_mul_f32 v228, s71, v228
PX 12,224,216
buffer_store_dwordx2 a[100:101], v242, s[52:55], s59 offen offset:1536 nt
ds_read_b128 v[248:251], v254 offset:0
PX 8,220,216
s_waitcnt lgkmcnt(3)
RT 128,247,0
v_mul_f32 v230, s71, v230
PX 28,224,220
RT 142,247,1600
CV 91,107,91
v_mul_f32 v233, s71, v233
PX 24,220,220
buffer_load_dwordx4 v237, s[44:47], s56 offen offset:0 lds
CV 69,100,69
v_mul_f32 v232, s71, v232
v_mul_f32 v235, s71, v235
PX 4,216,216
s_add_u32 s57, s64, 49152
v_exp_f32 v229, v229
PX 0,212,216
v_exp_f32 v228, v228
v_exp_f32 v235, v235
PX 20,216,220
RT 132,247,320
CV 76,76,92
v_exp_f32 v233, v233
PX 16,212,220
s_waitcnt lgkmcnt(1)
ds_write_b128 v246, v[68:71] offset:0
v_exp_f32 v230, v230
v_pk_mul_f32 v[116:117], v[116:117], v[248:249]
v_pk_mul_f32 v[118:119], v[118:119], v[250:251]
v_pk_mul_f32 v[132:133], v[132:133], v[248:249]
v_pk_mul_f32 v[134:135], v[134:135], v[250:251]
v_pk_mul_f32 v[148:149], v[148:149], v[248:249]
v_pk_mul_f32 v[150:151], v[150:151], v[250:251]
v_pk_mul_f32 v[164:165], v[164:165], v[248:249]
v_pk_mul_f32 v[166:167], v[166:167], v[250:251]
v_exp_f32 v232, v232
PX 32,212,224
buffer_store_dwordx2 a[102:103], v242, s[52:55], s59 offen offset:2304 nt
ds_read_b128 v[248:251], v254 offset:64
PX 36,216,224
RT 220,240,25616
CV 182,228,229
PX 48,212,228
CV 183,230,231
CV 116,116,132
PX 52,216,228
s_add_u32 m0, s60, 13184
RT 136,247,0
CV 92,78,94
v_mul_f32 v234, s71, v234
QC 228,212,68
buffer_load_dwordx4 v237, s[44:47], s56 offen offset:128 lds
RT 140,247,320
QK 228,208,64
s_add_u32 m0, s60, 14080
RT 222,240,31760
CV 132,118,134
QK 228,204,60
RT 218,240,31744
CV 134,119,135
v_exp_f32 v234, v234
QK 228,200,56
s_waitcnt lgkmcnt(4)
RT 138,247,1280
ds_write_b128 v246, v[72:75] offset:0
CV 94,79,95
v_pk_mul_f32 v[120:121], v[120:121], v[248:249]
v_pk_mul_f32 v[122:123], v[122:123], v[250:251]
v_pk_mul_f32 v[136:137], v[136:137], v[248:249]
v_pk_mul_f32 v[138:139], v[138:139], v[250:251]
CV 186,232,233
QK 228,196,52
RT 144,247,0
CV 119,149,165
CV 120,120,136
QK 228,192,48
RT 216,240,25600
ds_write_b128 v246, v[88:91] offset:1280
CV 95,111,115
v_pk_mul_f32 v[152:153], v[152:153], v[248:249]
v_pk_mul_f32 v[154:155], v[154:155], v[250:251]
v_pk_mul_f32 v[168:169], v[168:169], v[248:249]
v_pk_mul_f32 v[170:171], v[170:171], v[250:251]
CV 187,234,235
QC 232,192,72
ds_read_b128 v[248:251], v254 offset:128
CV 78,77,93
QK 232,196,76
RQ 192,238,30720
CV 79,109,113
QK 232,200,80
RQ 200,238,31744
CV 118,117,133
QK 232,204,84
RT 148,247,320
v_mul_f32 v229, s71, v229
QK 232,208,88
RQ 196,239,30720
CV 93,110,114
v_mul_f32 v230, s71, v230
v_mul_f32 v231, s71, v231
QK 232,212,92
RQ 212,239,32768
CV 77,108,112
v_exp_f32 v229, v229
PX 40,220,224
RQ 204,239,31744
v_mul_f32 v228, s71, v228
PX 44,224,224
s_waitcnt lgkmcnt(4)
RQ 208,238,32768
ds_write_b128 v246, v[76:79] offset:0
v_pk_mul_f32 v[124:125], v[124:125], v[248:249]
v_pk_mul_f32 v[140:141], v[140:141], v[248:249]
v_pk_mul_f32 v[142:143], v[142:143], v[250:251]
v_pk_mul_f32 v[156:157], v[156:157], v[248:249]
v_pk_mul_f32 v[172:173], v[172:173], v[248:249]
v_pk_mul_f32 v[174:175], v[174:175], v[250:251]
v_exp_f32 v231, v231
PX 56,220,228
RT 146,247,1280
v_exp_f32 v228, v228
CV 124,124,140
PX 60,224,228
RT 150,247,1600
ds_write_b128 v246, v[92:95] offset:1280
v_pk_mul_f32 v[126:127], v[126:127], v[250:251]
v_pk_mul_f32 v[158:159], v[158:159], v[250:251]
v_mul_f32 v232, s71, v232
PX 76,224,216
buffer_load_dwordx4 v237, s[44:47], s56 offen offset:256 lds
PX 72,220,216
s_add_u32 m0, s60, 62336
CV 136,122,138
v_mul_f32 v233, s71, v233
v_mul_f32 v235, s71, v235
v_exp_f32 v232, v232
PX 92,224,220
v_exp_f32 v230, v230
PX 88,220,220
CV 138,123,139
CV 190,228,229
PX 68,216,216
s_waitcnt vmcnt(21) lgkmcnt(4)
v_exp_f32 v233, v233
PX 64,212,216
s_barrier
RT 152,247,0
PX 84,216,220
L_s34_%=:
RT 224,240,36864
CV 191,230,231
PX 80,212,220
RT 218,241,43008
CV 139,155,171
v_mul_f32 v234, s71, v234
QC 228,212,20
buffer_load_dwordx4 v237, s[48:51], s77 offen offset:128 lds
RT 158,247,1600
CV 194,232,233
QK 228,208,16
v_exp_f32 v234, v234
QK 228,204,12
s_add_u32 s59, s69, 6144
v_exp_f32 v235, v235
QK 228,200,8
RT 226,240,43008
CV 135,151,167
QK 228,196,4
s_add_u32 m0, s60, 63232
RT 230,240,43024
CV 133,150,166
QK 228,192,0
RT 216,241,36864
CV 140,126,142
CV 195,234,235
QC 232,192,24
ds_read_b128 v[248:251], v254 offset:192
CV 122,121,137
QK 232,196,28
RT 228,240,36880
CV 117,148,164
QK 232,200,32
buffer_load_dwordx4 v237, s[48:51], s77 offen offset:256 lds
QK 232,204,36
RT 220,241,36880
v_mul_f32 v231, s71, v231
QK 232,208,40
CV 126,125,141
v_mul_f32 v228, s71, v228
QK 232,212,44
s_add_u32 m0, s60, 73728
RT 222,241,43024
v_mul_f32 v229, s71, v229
v_smfmac_f32_16x16x64_bf16 a[232:235], a[236:239], v[180:187], v236
s_waitcnt lgkmcnt(3)
RT 156,247,320
v_mul_f32 v230, s71, v230
PW 0,180,224
v_exp_f32 v229, v229
PW 4,184,224
s_waitcnt lgkmcnt(3)
ds_write_b128 v246, v[116:119] offset:0
v_pk_mul_f32 v[176:177], v[176:177], v[248:249]
CV 123,153,169
PW 16,180,228
buffer_store_dwordx2 a[104:105], v242, s[52:55], s59 offen offset:0 nt
v_mul_f32 v232, s71, v232
PW 20,184,228
v_exp_f32 v231, v231
PW 8,188,224
RT 154,247,1280
ds_write_b128 v246, v[132:135] offset:1280
v_pk_mul_f32 v[178:179], v[178:179], v[250:251]
v_mul_f32 v234, s71, v234
PW 12,192,224
v_exp_f32 v228, v228
PW 24,188,228
v_exp_f32 v230, v230
PW 28,192,228
RT 162,247,1280
CV 196,228,229
PW 44,192,216
RT 166,247,1600
v_mul_f32 v233, s71, v233
PW 40,188,216
s_waitcnt lgkmcnt(3)
CV 137,154,170
v_mul_f32 v235, s71, v235
PW 60,192,220
buffer_load_dwordx4 v237, s[48:51], s57 offen offset:0 lds
RT 228,240,37904
PW 56,188,220
s_add_u32 m0, s60, 102400
RT 224,240,37888
ds_write_b128 v246, v[136:139] offset:1280
CV 142,127,143
v_pk_mul_f32 v[128:129], v[128:129], v[248:249]
v_pk_mul_f32 v[130:131], v[130:131], v[250:251]
v_pk_mul_f32 v[144:145], v[144:145], v[248:249]
v_pk_mul_f32 v[146:147], v[146:147], v[250:251]
v_pk_mul_f32 v[160:161], v[160:161], v[248:249]
CV 197,230,231
QC 228,212,68
v_exp_f32 v232, v232
QK 228,208,64
v_exp_f32 v233, v233
QK 228,204,60
v_exp_f32 v235, v235
QK 228,200,56
buffer_store_dwordx2 a[106:107], v242, s[52:55], s59 offen offset:768 nt
RT 164,247,320
)"
R"(
QK 228,196,52
RT 226,240,44032
v_exp_f32 v234, v234
QK 228,192,48
RT 230,240,44048
CV 121,152,168
CV 200,232,233
CV 201,234,235
QC 232,192,72
RT 170,247,1280
CV 143,159,175
QK 232,196,76
RQ 192,238,36864
CV 141,158,174
QK 232,200,80
buffer_load_dwordx4 v237, s[48:51], s87 offen offset:0 lds
QK 232,204,84
s_add_u32 s56, s64, 67584
v_mul_f32 v231, s71, v231
v_exp_f32 v231, v231
QK 232,208,88
RT 160,247,0
CV 128,128,144
v_mul_f32 v229, s71, v229
QK 232,212,92
s_add_u32 m0, s60, 24576
RQ 212,239,38912
v_mul_f32 v230, s71, v230
PW 36,184,216
RQ 204,239,37888
CV 144,130,146
PW 32,180,216
ds_write_b128 v246, v[120:123] offset:0
PW 52,184,220
v_exp_f32 v229, v229
PW 48,180,220
s_waitcnt lgkmcnt(3)
v_exp_f32 v230, v230
PW 64,180,224
RQ 208,238,38912
v_mul_f32 v228, s71, v228
PW 68,184,224
buffer_load_dwordx4 v237, s[44:47], s56 offen offset:0 lds
RQ 196,239,36864
PW 80,180,228
RQ 200,238,37888
v_mul_f32 v232, s71, v232
v_mul_f32 v234, s71, v234
PW 84,184,228
RT 174,247,1600
v_mul_f32 v233, s71, v233
PW 72,188,224
v_exp_f32 v228, v228
PW 76,192,224
v_mul_f32 v235, s71, v235
v_exp_f32 v232, v232
PW 88,188,228
s_waitcnt lgkmcnt(3)
RT 172,247,320
CV 204,228,229
PW 92,192,228
s_add_u32 m0, s60, 103296
RT 218,241,31744
CV 205,230,231
QC 228,212,20
v_exp_f32 v235, v235
QK 228,208,16
buffer_load_dwordx4 v237, s[48:51], s87 offen offset:128 lds
RT 168,247,0
QK 228,204,12
s_waitcnt lgkmcnt(3)
v_exp_f32 v234, v234
QK 228,200,8
v_exp_f32 v233, v233
QK 228,196,4
RT 224,240,26624
CV 208,232,233
QK 228,192,0
RT 216,241,25600
CV 209,234,235
QC 232,192,24
RT 222,241,31760
CV 130,129,145
QK 232,196,28
s_add_u32 m0, s60, 25472
RT 220,241,25616
CV 129,160,176
QK 232,200,32
RT 230,240,32784
CV 146,131,147
QK 232,204,36
CV 125,156,172
v_mul_f32 v229, s71, v229
v_mul_f32 v230, s71, v230
QK 232,208,40
RT 228,240,26640
v_mul_f32 v228, s71, v228
QK 232,212,44
buffer_load_dwordx4 v237, s[44:47], s56 offen offset:128 lds
v_smfmac_f32_16x16x64_bf16 a[232:235], a[240:243], v[188:195], v236
s_waitcnt lgkmcnt(3)
v_exp_f32 v229, v229
PX 108,224,216
RT 226,240,32768
v_mul_f32 v231, s71, v231
PX 104,220,216
s_waitcnt lgkmcnt(2)
ds_write_b128 v246, v[140:143] offset:1280
PX 112,224,220
v_exp_f32 v231, v231
v_mfma_f32_16x16x32_bf16 a[252:255], v[220:223], a[220:223], 0
CV 127,157,173
v_mul_f32 v235, s71, v235
PX 100,216,216
v_exp_f32 v228, v228
PX 96,212,216
RT 182,247,1600
v_mul_f32 v233, s71, v233
v_mfma_f32_16x16x32_bf16 a[248:251], v[216:219], a[220:223], 0
buffer_store_dwordx2 a[108:109], v242, s[52:55], s59 offen offset:1536 nt
RT 216,241,26624
v_mul_f32 v232, s71, v232
v_mfma_f32_16x16x32_bf16 a[244:247], v[212:215], a[220:223], 0
s_waitcnt lgkmcnt(2)
RT 178,247,1280
v_exp_f32 v230, v230
PX 116,212,224
ds_write_b128 v246, v[124:127] offset:0
v_pk_mul_f32 v[162:163], v[162:163], v[250:251]
CV 198,228,229
PX 120,216,224
s_add_u32 m0, s60, 26368
CV 199,230,231
L_c16_%=:
s_cbranch_execz L_ret_%=
v_mul_f32 v234, s71, v234
PX 132,212,228
v_exp_f32 v234, v234
PX 136,216,228
v_exp_f32 v233, v233
QC 228,212,68
buffer_store_dwordx2 a[110:111], v242, s[52:55], s59 offen offset:2304 nt
RT 220,241,26640
QK 228,208,64
v_exp_f32 v232, v232
QK 228,204,60
RT 222,241,32784
CV 131,161,177
QK 228,200,56
RT 176,247,0
v_exp_f32 v235, v235
QK 228,196,52
RT 218,241,32768
CV 203,234,235
QK 228,192,48
RT 180,247,320
CV 202,232,233
QC 232,192,72
RQ 192,238,43008
ds_write_b128 v246, v[128:131] offset:0
QK 232,196,76
buffer_load_dwordx4 v237, s[44:47], s56 offen offset:256 lds
QK 232,200,80
CV 145,162,178
CV 147,163,179
QK 232,204,84
RQ 200,238,44032
v_cmp_gt_u32 s[78:79], s74, v252
QK 232,208,88
RQ 208,238,45056
v_mul_f32 v229, s71, v229
QK 232,212,92
RQ 196,239,43008
v_mul_f32 v228, s71, v228
PX 124,220,224
RQ 204,239,44032
v_mul_f32 v230, s71, v230
PX 128,224,224
RQ 212,239,45056
v_mul_f32 v231, s71, v231
PX 140,220,228
s_add_u32 m0, s60, 104192
ds_write_b128 v246, v[144:147] offset:1280
PX 144,224,228
s_waitcnt lgkmcnt(4)
v_exp_f32 v229, v229
PX 160,224,216
buffer_load_dwordx4 v237, s[48:51], s87 offen offset:256 lds
PX 156,220,216
s_add_u32 m0, s60, 74624
v_exp_f32 v228, v228
PX 176,224,220
s_add_u32 s59, s69, 12288
v_exp_f32 v230, v230
PX 172,220,220
s_add_u32 s77, s64, 61440
CV 206,228,229
v_mul_f32 v232, s71, v232
PX 152,216,216
s_waitcnt vmcnt(25) lgkmcnt(1)
v_exp_f32 v231, v231
PX 148,212,216
s_barrier
PX 168,216,220
L_s35_%=:
CV 207,230,231
v_mul_f32 v235, s71, v235
PX 164,212,220
v_mul_f32 v233, s71, v233
v_mul_f32 v234, s71, v234
QC 228,212,20
buffer_load_dwordx4 v237, s[48:51], s57 offen offset:128 lds
QK 228,208,16
s_add_u32 s56, s64, 178176
v_exp_f32 v232, v232
QK 228,204,12
v_exp_f32 v233, v233
QK 228,200,8
v_exp_f32 v234, v234
QK 228,196,4
v_exp_f32 v235, v235
QK 228,192,0
s_add_u32 m0, s60, 151552
CV 210,232,233
CV 211,234,235
QC 232,192,24
RT 220,241,1040
QK 232,196,28
RT 228,241,1024
QK 232,200,32
buffer_load_dwordx4 v237, s[44:47], s56 offen offset:0 lds
QK 232,204,36
v_mul_f32 v229, s71, v229
v_mul_f32 v231, s71, v231
QK 232,208,40
v_mul_f32 v228, s71, v228
v_mul_f32 v230, s71, v230
QK 232,212,44
v_exp_f32 v229, v229
QC 248,212,68
v_exp_f32 v231, v231
QK 248,208,64
v_exp_f32 v230, v230
QK 248,204,60
v_exp_f32 v228, v228
QK 248,200,56
s_add_u32 m0, s60, 75520
RT 226,241,44032
CV 212,228,229
QK 248,196,52
buffer_load_dwordx4 v237, s[48:51], s57 offen offset:256 lds
QK 248,192,48
RT 224,241,37888
CV 213,230,231
QC 228,192,72
s_add_u32 m0, s60, 152448
RT 230,241,7168
QK 228,196,76
RT 216,241,37904
v_mul_f32 v232, s71, v232
QK 228,200,80
RQ 200,238,1024
QK 228,204,84
RT 222,241,7184
QK 228,208,88
buffer_store_dwordx2 a[112:113], v242, s[52:55], s59 offen offset:0 nt
RT 218,241,44048
QK 228,212,92
v_smfmac_f32_16x16x64_bf16 a[232:235], a[236:239], v[196:203], v236
s_waitcnt lgkmcnt(4)
buffer_load_dwordx4 v237, s[44:47], s56 offen offset:128 lds
PW 96,180,224
v_mul_f32 v234, s71, v234
v_mul_f32 v249, s71, v249
PW 96,196,228
v_exp_f32 v232, v232
PW 100,184,224
s_add_u32 m0, s60, 86016
RQ 192,238,0
v_mul_f32 v248, s71, v248
PW 100,200,228
buffer_store_dwordx2 a[114:115], v242, s[52:55], s59 offen offset:768 nt
v_mul_f32 v229, s71, v229
PW 104,188,224
v_mul_f32 v235, s71, v235
v_mul_f32 v251, s71, v251
PW 104,204,228
v_exp_f32 v234, v234
PW 108,192,224
RT 226,240,45056
PW 108,208,228
s_waitcnt lgkmcnt(2)
RT 224,240,38912
v_mul_f32 v233, s71, v233
PW 112,192,216
RT 228,240,2048
v_mul_f32 v250, s71, v250
PW 112,208,220
RT 230,240,8192
v_mul_f32 v228, s71, v228
v_mfma_f32_16x16x32_bf16 a[252:255], v[188:191], a[216:219], a[252:255]
buffer_load_dwordx4 v237, s[48:51], s77 offen offset:0 lds
v_mfma_f32_16x16x32_bf16 a[252:255], v[204:207], a[220:223], a[252:255]
v_exp_f32 v235, v235
v_mfma_f32_16x16x32_bf16 a[248:251], v[184:187], a[216:219], a[248:251]
s_add_u32 m0, s60, 153344
v_exp_f32 v251, v251
v_mfma_f32_16x16x32_bf16 a[248:251], v[200:203], a[220:223], a[248:251]
v_exp_f32 v233, v233
v_mfma_f32_16x16x32_bf16 a[244:247], v[180:183], a[216:219], a[244:247]
RQ 204,239,1024
v_mfma_f32_16x16x32_bf16 a[244:247], v[196:199], a[220:223], a[244:247]
s_waitcnt lgkmcnt(1)
RQ 208,238,2048
PW 116,180,224
RQ 212,239,2048
PW 116,196,228
v_exp_f32 v248, v248
PW 120,184,224
buffer_load_dwordx4 v237, s[44:47], s56 offen offset:256 lds
PW 120,200,228
RQ 196,239,0
v_mul_f32 v231, s71, v231
PW 124,188,224
s_add_u32 m0, s60, 36864
v_exp_f32 v249, v249
PW 124,204,228
CV 216,232,233
CV 217,234,235
PW 128,192,224
RT 216,240,38928
CV 220,248,249
PW 128,208,228
s_waitcnt lgkmcnt(2)
RT 222,240,8208
v_mul_f32 v230, s71, v230
QC 232,212,20
s_add_u32 s56, s64, 79872
v_exp_f32 v250, v250
QK 232,208,16
RT 220,240,2064
CV 221,250,251
QK 232,204,12
buffer_load_dwordx4 v237, s[44:47], s56 offen offset:0 lds
QK 232,200,8
s_waitcnt lgkmcnt(3)
v_exp_f32 v231, v231
QK 232,196,4
v_exp_f32 v229, v229
QK 232,192,0
RT 218,240,45072
QC 248,192,24
s_add_u32 m0, s60, 37760
QK 248,196,28
v_exp_f32 v230, v230
QK 248,200,32
v_exp_f32 v228, v228
QK 248,204,36
CV 225,230,231
v_mul_f32 v232, s71, v232
QK 248,208,40
buffer_load_dwordx4 v237, s[44:47], s56 offen offset:128 lds
QK 248,212,44
s_add_u32 m0, s60, 38656
CV 224,228,229
v_mul_f32 v234, s71, v234
QC 228,212,68
v_mul_f32 v233, s71, v233
v_mul_f32 v235, s71, v235
QK 228,208,64
v_exp_f32 v235, v235
QK 228,204,60
v_exp_f32 v234, v234
QK 228,200,56
v_exp_f32 v233, v233
QK 228,196,52
v_exp_f32 v232, v232
QK 228,192,48
CV 214,232,233
CV 215,234,235
QC 232,192,72
buffer_load_dwordx4 v237, s[44:47], s56 offen offset:256 lds
QK 232,196,76
RT 224,241,38912
QK 232,200,80
s_add_u32 s56, s64, 92160
v_mul_f32 v248, s71, v248
QK 232,204,84
buffer_store_dwordx2 a[116:117], v242, s[52:55], s59 offen offset:1536 nt
RQ 196,239,6144
QK 232,208,88
RT 228,241,2048
v_mul_f32 v229, s71, v229
QK 232,212,92
RT 230,241,8192
v_smfmac_f32_16x16x64_bf16 a[232:235], a[240:243], v[204:211], v236
s_waitcnt lgkmcnt(4)
RT 226,241,45056
v_mul_f32 v250, s71, v250
PW 144,192,216
)"
R"(
s_add_u32 m0, s60, 86912
RQ 192,238,6144
PW 144,208,220
v_exp_f32 v229, v229
PW 140,188,216
v_mul_f32 v233, s71, v233
PW 140,204,220
RQ 200,238,7168
v_mul_f32 v234, s71, v234
PW 136,184,216
RQ 212,239,8192
v_mul_f32 v249, s71, v249
PW 136,200,220
buffer_store_dwordx2 a[118:119], v242, s[52:55], s59 offen offset:2304 nt
RQ 204,239,7168
PW 132,180,216
RT 218,241,45072
v_mul_f32 v231, s71, v231
PW 132,196,220
s_waitcnt lgkmcnt(4)
RT 216,241,38928
v_mul_f32 v235, s71, v235
PW 148,180,224
RT 220,241,2064
v_mul_f32 v228, s71, v228
PW 148,196,228
s_add_u32 s59, s69, 18432
RT 222,241,8208
v_mul_f32 v230, s71, v230
PW 152,184,224
RQ 208,238,8192
v_mul_f32 v251, s71, v251
PW 152,200,228
v_exp_f32 v230, v230
PW 156,188,224
RT 186,247,1280
PW 156,204,228
v_exp_f32 v231, v231
PW 160,192,224
v_exp_f32 v251, v251
PW 160,208,228
s_waitcnt lgkmcnt(2)
v_exp_f32 v249, v249
PW 176,192,216
v_exp_f32 v235, v235
PW 176,208,220
v_exp_f32 v228, v228
PW 172,188,216
v_exp_f32 v233, v233
PW 172,204,220
CV 222,228,229
CV 223,230,231
PW 168,184,216
s_waitcnt vmcnt(25) lgkmcnt(1)
v_exp_f32 v248, v248
PW 168,200,220
s_barrier
PW 164,180,216
L_s36_%=:
s_mov_b64 exec, 0
s_cmp_eq_u32 s86, 0
s_cbranch_scc1 L_c17_%=
s_cmp_eq_u32 s86, 1
s_cbranch_scc1 L_c18_%=
s_cmp_eq_u32 s86, 2
s_cbranch_scc1 L_c19_%=
s_branch L_c20_%=
L_ret2_%=:
s_mov_b64 exec, -1
PW 164,196,220
buffer_load_dwordx4 v237, s[48:51], s77 offen offset:128 lds
QC 228,212,20
v_exp_f32 v234, v234
QK 228,208,16
v_mul_f32 v232, s71, v232
QK 228,204,12
v_exp_f32 v232, v232
QK 228,200,8
RT 226,240,6144
CV 218,248,249
QK 228,196,4
RT 216,240,16
CV 227,234,235
QK 228,192,0
RT 224,240,0
CV 226,232,233
QC 232,192,24
v_exp_f32 v250, v250
QK 232,196,28
s_add_u32 s57, s64, 73728
CV 219,250,251
QK 232,200,32
QK 232,204,36
buffer_store_dwordx2 a[120:121], v242, s[52:55], s59 offen offset:0 nt
v_mul_f32 v228, s71, v228
QK 232,208,40
RT 230,240,18432
v_mul_f32 v231, s71, v231
QK 232,212,44
v_exp_f32 v231, v231
QC 248,212,68
v_exp_f32 v228, v228
QK 248,208,64
RT 222,240,18448
v_mul_f32 v230, s71, v230
QK 248,204,60
s_add_u32 m0, s60, 87808
v_exp_f32 v230, v230
QK 248,200,56
v_mul_f32 v229, s71, v229
CV 181,230,231
QK 248,196,52
v_exp_f32 v229, v229
QK 248,192,48
CV 180,228,229
v_mul_f32 v235, s71, v235
QC 228,192,72
RT 228,240,12288
v_mul_f32 v233, s71, v233
QK 228,196,76
buffer_load_dwordx4 v237, s[48:51], s77 offen offset:256 lds
QK 228,200,80
v_exp_f32 v235, v235
QK 228,204,84
RT 218,240,6160
v_mul_f32 v248, s71, v248
QK 228,208,88
v_exp_f32 v233, v233
QK 228,212,92
RT 220,240,12304
v_smfmac_f32_16x16x64_bf16 a[232:235], a[236:239], v[212:219], v236
s_waitcnt lgkmcnt(2)
RQ 200,238,13312
v_mul_f32 v249, s71, v249
PW 0,196,224
RQ 192,238,12288
v_mul_f32 v251, s71, v251
PW 0,212,228
buffer_store_dwordx2 a[122:123], v242, s[52:55], s59 offen offset:768 nt
RQ 204,239,13312
PW 4,200,224
v_mul_f32 v232, s71, v232
PW 4,216,228
PW 8,204,224
RQ 212,239,14336
v_mul_f32 v230, s71, v230
PW 8,220,228
v_mul_f32 v231, s71, v231
PW 12,208,224
s_waitcnt lgkmcnt(2)
RT 224,241,0
v_mul_f32 v234, s71, v234
PW 12,224,228
s_add_u32 m0, s60, 50944
RT 228,241,12288
v_mul_f32 v229, s71, v229
PW 28,208,216
RT 226,241,6144
v_mul_f32 v250, s71, v250
PW 28,224,220
RT 230,241,18432
PW 24,204,216
buffer_load_dwordx4 v237, s[48:51], s57 offen offset:256 lds
PW 24,220,220
v_exp_f32 v232, v232
PW 20,200,216
v_exp_f32 v234, v234
PW 20,216,220
RQ 208,238,14336
v_mul_f32 v228, s71, v228
PW 16,196,216
s_waitcnt lgkmcnt(2)
v_exp_f32 v250, v250
PW 16,212,220
s_add_u32 m0, s60, 0
RQ 196,239,12288
CV 184,232,233
PW 32,196,224
s_waitcnt lgkmcnt(2)
v_exp_f32 v228, v228
PW 32,212,228
v_exp_f32 v249, v249
PW 36,200,224
buffer_load_dwordx4 v237, s[44:47], s56 offen offset:0 lds
PW 36,216,228
CV 185,234,235
PW 40,204,224
v_exp_f32 v231, v231
PW 40,220,228
v_exp_f32 v251, v251
PW 44,208,224
s_add_u32 m0, s60, 896
v_exp_f32 v229, v229
PW 44,224,228
CV 189,250,251
CV 192,228,229
QC 232,212,20
s_waitcnt lgkmcnt(0)
QK 232,208,16
v_exp_f32 v230, v230
QK 232,204,12
buffer_load_dwordx4 v237, s[44:47], s56 offen offset:128 lds
QK 232,200,8
RT 218,241,6160
QK 232,196,4
v_exp_f32 v248, v248
QK 232,192,0
RT 216,241,16
CV 188,248,249
QC 248,192,24
RT 224,240,1024
QK 248,196,28
QK 248,200,32
RT 220,241,12304
CV 193,230,231
QK 248,204,36
s_add_u32 m0, s60, 1792
v_mul_f32 v233, s71, v233
v_mul_f32 v234, s71, v234
QK 248,208,40
v_exp_f32 v233, v233
QK 248,212,44
buffer_load_dwordx4 v237, s[44:47], s56 offen offset:256 lds
QC 228,212,68
v_mul_f32 v232, s71, v232
v_mul_f32 v235, s71, v235
QK 228,208,64
v_exp_f32 v232, v232
QK 228,204,60
v_exp_f32 v235, v235
QK 228,200,56
s_add_u32 s56, s64, 104448
v_exp_f32 v234, v234
QK 228,196,52
buffer_store_dwordx2 a[124:125], v242, s[52:55], s59 offen offset:1536 nt
CV 182,232,233
QK 228,192,48
RT 222,241,18448
CV 183,234,235
QC 232,192,72
RT 228,240,13312
v_mul_f32 v251, s71, v251
QK 232,196,76
buffer_store_dwordx2 a[126:127], v242, s[52:55], s59 offen offset:2304 nt
QK 232,200,80
v_exp_f32 v251, v251
QK 232,204,84
RT 226,240,7168
QK 232,208,88
v_mul_f32 v248, s71, v248
QK 232,212,92
RT 230,240,19456
v_mul_f32 v229, s71, v229
v_smfmac_f32_16x16x64_bf16 a[232:235], a[240:243], v[220:227], v236
s_waitcnt lgkmcnt(2)
RQ 204,239,19456
v_mul_f32 v228, s71, v228
PW 60,208,216
RQ 196,239,18432
PW 60,224,220
s_add_u32 m0, s60, 50048
PW 56,204,216
v_exp_f32 v229, v229
PW 56,220,220
RQ 192,238,18432
v_mul_f32 v231, s71, v231
PW 52,200,216
RQ 212,239,20480
v_mul_f32 v250, s71, v250
PW 52,216,220
RQ 200,238,19456
PW 48,196,216
RT 216,240,1040
v_mul_f32 v233, s71, v233
PW 48,212,220
s_waitcnt lgkmcnt(4)
s_add_u32 s77, s64, 86016
RT 220,240,13328
PW 64,196,224
RT 218,240,7184
v_mul_f32 v232, s71, v232
PW 64,212,228
s_add_u32 s59, s69, 0
RT 222,240,19472
PW 68,200,224
v_mul_f32 v249, s71, v249
v_mul_f32 v230, s71, v230
PW 68,216,228
RQ 208,238,20480
v_mul_f32 v234, s71, v234
PW 72,204,224
v_exp_f32 v230, v230
PW 72,220,228
v_mul_f32 v235, s71, v235
PW 76,208,224
v_exp_f32 v228, v228
PW 76,224,228
s_waitcnt lgkmcnt(1)
v_exp_f32 v231, v231
PW 92,208,216
CV 191,230,231
PW 92,224,220
v_exp_f32 v250, v250
PW 88,204,216
v_exp_f32 v233, v233
PW 88,220,220
v_exp_f32 v235, v235
PW 84,200,216
s_waitcnt vmcnt(20) lgkmcnt(0)
v_exp_f32 v234, v234
PW 84,216,220
s_barrier
PW 80,196,216
L_s37_%=:
buffer_load_dwordx4 v237, s[48:51], s57 offen offset:128 lds
PW 80,212,220
CV 187,250,251
CV 190,228,229
QC 228,212,20
v_exp_f32 v232, v232
QK 228,208,16
s_add_u32 m0, s60, 49152
RT 230,241,31744
CV 195,234,235
QK 228,204,12
RT 226,241,19456
CV 194,232,233
QK 228,200,8
RT 218,241,19472
QK 228,196,4
v_exp_f32 v248, v248
QK 228,192,0
RT 224,241,13312
QC 232,192,24
buffer_load_dwordx4 v237, s[48:51], s57 offen offset:0 lds
QK 232,196,28
v_exp_f32 v249, v249
QK 232,200,32
RT 184,247,0
CV 186,248,249
QK 232,204,36
v_mul_f32 v228, s71, v228
v_mul_f32 v231, s71, v231
L_c17_%=:
s_cbranch_execz L_c21_%=
QK 232,208,40
RT 220,241,25616
v_mul_f32 v229, s71, v229
QK 232,212,44
v_exp_f32 v228, v228
QC 248,212,68
buffer_store_dwordx2 a[128:129], v242, s[52:55], s59 offen offset:128 nt
v_mul_f32 v230, s71, v230
QK 248,208,64
v_exp_f32 v229, v229
QK 248,204,60
v_exp_f32 v231, v231
QK 248,200,56
s_add_u32 m0, s60, 61440
v_exp_f32 v230, v230
QK 248,196,52
CV 196,228,229
CV 197,230,231
QK 248,192,48
buffer_load_dwordx4 v237, s[48:51], s77 offen offset:0 lds
QC 228,192,72
RT 228,241,25600
v_mul_f32 v234, s71, v234
QK 228,196,76
v_exp_f32 v234, v234
QK 228,200,80
RT 216,241,13328
QK 228,204,84
RT 222,241,31760
QK 228,208,88
v_mul_f32 v235, s71, v235
v_mul_f32 v249, s71, v249
QK 228,212,92
RQ 208,238,26624
v_mul_f32 v233, s71, v233
v_smfmac_f32_16x16x64_bf16 a[232:235], a[236:239], v[180:187], v236
s_waitcnt lgkmcnt(3)
RQ 204,239,25600
PW 96,212,224
s_add_u32 m0, s60, 12288
v_exp_f32 v249, v249
PW 96,180,228
buffer_store_dwordx2 a[130:131], v242, s[52:55], s59 offen offset:896 nt
PW 100,216,224
RQ 192,238,24576
v_mul_f32 v251, s71, v251
PW 100,184,228
v_mul_f32 v248, s71, v248
PW 104,220,224
v_mul_f32 v232, s71, v232
PW 104,188,228
RQ 212,239,26624
PW 108,224,224
RT 224,240,14336
PW 108,192,228
s_waitcnt lgkmcnt(3)
RT 226,240,20480
PW 112,224,216
RT 230,240,32768
v_mul_f32 v229, s71, v229
PW 112,192,220
RT 228,240,26624
v_mul_f32 v231, s71, v231
v_mfma_f32_16x16x32_bf16 a[252:255], v[220:223], a[216:219], a[252:255]
)"
R"(
buffer_load_dwordx4 v237, s[44:47], s56 offen offset:0 lds
v_mfma_f32_16x16x32_bf16 a[252:255], v[188:191], a[220:223], a[252:255]
v_exp_f32 v251, v251
v_mfma_f32_16x16x32_bf16 a[248:251], v[216:219], a[216:219], a[248:251]
v_exp_f32 v232, v232
v_mfma_f32_16x16x32_bf16 a[248:251], v[184:187], a[220:223], a[248:251]
s_add_u32 m0, s60, 13184
v_exp_f32 v233, v233
v_mfma_f32_16x16x32_bf16 a[244:247], v[212:215], a[216:219], a[244:247]
RQ 196,239,24576
v_mul_f32 v250, s71, v250
v_mfma_f32_16x16x32_bf16 a[244:247], v[180:183], a[220:223], a[244:247]
s_waitcnt lgkmcnt(1)
PW 116,212,224
RT 220,240,26640
v_mul_f32 v230, s71, v230
PW 116,180,228
RT 222,240,32784
v_mul_f32 v228, s71, v228
PW 120,216,224
v_exp_f32 v235, v235
PW 120,184,228
RQ 200,238,25600
CV 201,234,235
PW 124,220,224
buffer_load_dwordx4 v237, s[44:47], s56 offen offset:128 lds
PW 124,188,228
CV 200,232,233
PW 128,224,224
v_exp_f32 v229, v229
PW 128,192,228
v_exp_f32 v231, v231
QC 232,212,20
s_add_u32 m0, s60, 14080
v_exp_f32 v250, v250
QK 232,208,16
v_exp_f32 v248, v248
QK 232,204,12
s_waitcnt lgkmcnt(0)
v_exp_f32 v228, v228
QK 232,200,8
RT 218,240,20496
CV 205,250,251
QK 232,196,4
buffer_load_dwordx4 v237, s[44:47], s56 offen offset:256 lds
QK 232,192,0
CV 204,248,249
CV 208,228,229
QC 248,192,24
v_exp_f32 v230, v230
QK 248,196,28
s_or_b64 s[78:79], s[78:79], vcc
s_cselect_b32 s73, 2, 0
QK 248,200,32
RT 228,241,26624
CV 209,230,231
QK 248,204,36
v_mul_f32 v235, s71, v235
QK 248,208,40
v_exp_f32 v235, v235
QK 248,212,44
buffer_store_dwordx2 a[132:133], v242, s[52:55], s59 offen offset:1664 nt
RT 226,241,20480
QC 228,212,68
RT 216,240,14352
v_mul_f32 v234, s71, v234
QK 228,208,64
v_mul_f32 v232, s71, v232
v_mul_f32 v233, s71, v233
QK 228,204,60
v_exp_f32 v232, v232
QK 228,200,56
v_exp_f32 v233, v233
QK 228,196,52
s_add_u32 m0, s60, 62336
v_exp_f32 v234, v234
QK 228,192,48
CV 198,232,233
CV 199,234,235
QC 232,192,72
buffer_store_dwordx2 a[134:135], v242, s[52:55], s59 offen offset:2432 nt
QK 232,196,76
RT 224,241,14336
QK 232,200,80
RQ 200,238,31744
v_mul_f32 v249, s71, v249
QK 232,204,84
v_mul_f32 v251, s71, v251
QK 232,208,88
RT 230,241,32768
QK 232,212,92
RQ 208,238,32768
v_smfmac_f32_16x16x64_bf16 a[232:235], a[240:243], v[188:195], v236
s_waitcnt lgkmcnt(4)
RQ 204,239,31744
v_mul_f32 v248, s71, v248
PW 144,224,216
RQ 196,239,30720
PW 144,192,220
RQ 192,238,30720
v_mul_f32 v250, s71, v250
PW 140,220,216
v_mul_f32 v229, s71, v229
v_mul_f32 v235, s71, v235
PW 140,188,220
v_mul_f32 v231, s71, v231
PW 136,216,216
RQ 212,239,32768
PW 136,184,220
v_exp_f32 v231, v231
PW 132,212,216
RT 216,241,14352
v_mul_f32 v230, s71, v230
PW 132,180,220
s_waitcnt lgkmcnt(2)
RT 222,241,32784
PW 148,212,224
RT 218,241,20496
v_mul_f32 v233, s71, v233
PW 148,180,228
RT 220,241,26640
v_mul_f32 v228, s71, v228
PW 152,216,224
v_exp_f32 v229, v229
PW 152,184,228
v_exp_f32 v251, v251
PW 156,220,224
v_mul_f32 v232, s71, v232
v_mul_f32 v234, s71, v234
PW 156,188,228
v_exp_f32 v228, v228
PW 160,224,224
v_exp_f32 v230, v230
PW 160,192,228
s_waitcnt lgkmcnt(0)
v_exp_f32 v248, v248
PW 176,224,216
v_exp_f32 v234, v234
PW 176,192,220
s_add_u32 s57, s64, 98304
v_exp_f32 v233, v233
PW 172,220,216
CV 206,228,229
PW 172,188,220
v_exp_f32 v250, v250
PW 168,216,216
s_waitcnt vmcnt(19)
s_add_u32 s59, s69, 6144
CV 207,230,231
PW 168,184,220
s_barrier
PW 164,212,216
L_s38_%=:
v_exp_f32 v235, v235
PW 164,180,220
buffer_load_dwordx4 v237, s[48:51], s77 offen offset:128 lds
QC 228,212,20
RT 228,240,36864
QK 228,208,16
v_exp_f32 v232, v232
QK 228,204,12
CV 211,234,235
QK 228,200,8
v_exp_f32 v249, v249
QK 228,196,4
RT 230,240,43008
QK 228,192,0
CV 202,248,249
CV 210,232,233
QC 232,192,24
CV 203,250,251
QK 232,196,28
RT 226,240,30720
QK 232,200,32
buffer_store_dwordx2 a[136:137], v242, s[52:55], s59 offen offset:128 nt
QK 232,204,36
v_mul_f32 v229, s71, v229
QK 232,208,40
v_mul_f32 v228, s71, v228
v_mul_f32 v231, s71, v231
QK 232,212,44
v_mul_f32 v230, s71, v230
QC 248,212,68
s_add_u32 m0, s60, 63232
v_exp_f32 v230, v230
QK 248,208,64
s_or_b32 s72, s72, s73
v_exp_f32 v229, v229
QK 248,204,60
v_exp_f32 v228, v228
QK 248,200,56
v_exp_f32 v231, v231
QK 248,196,52
RT 224,240,24576
CV 212,228,229
QK 248,192,48
CV 213,230,231
v_mul_f32 v235, s71, v235
QC 228,192,72
buffer_load_dwordx4 v237, s[48:51], s77 offen offset:256 lds
QK 228,196,76
RT 222,240,43024
v_mul_f32 v234, s71, v234
QK 228,200,80
RT 218,240,30736
QK 228,204,84
RT 216,240,24592
v_mul_f32 v251, s71, v251
QK 228,208,88
v_exp_f32 v234, v234
QK 228,212,92
v_exp_f32 v251, v251
v_smfmac_f32_16x16x64_bf16 a[232:235], a[236:239], v[196:203], v236
s_waitcnt lgkmcnt(3)
RQ 204,239,37888
PW 0,180,224
v_mul_f32 v233, s71, v233
PW 0,196,228
s_add_u32 s56, s64, 116736
RT 220,240,36880
PW 4,184,224
s_add_u32 m0, s60, 75520
RQ 196,239,36864
v_mul_f32 v249, s71, v249
PW 4,200,228
buffer_store_dwordx2 a[138:139], v242, s[52:55], s59 offen offset:896 nt
v_mul_f32 v230, s71, v230
PW 8,188,224
RQ 200,238,37888
v_mul_f32 v228, s71, v228
PW 8,204,228
v_exp_f32 v249, v249
PW 12,192,224
RT 224,241,24576
PW 12,208,228
s_waitcnt lgkmcnt(3)
RT 230,241,43008
v_mul_f32 v250, s71, v250
PW 28,192,216
RT 226,241,30720
v_mul_f32 v231, s71, v231
PW 28,208,220
RT 228,241,36864
v_mul_f32 v232, s71, v232
PW 24,188,216
buffer_load_dwordx4 v237, s[48:51], s57 offen offset:256 lds
PW 24,204,220
v_exp_f32 v232, v232
PW 20,184,216
v_exp_f32 v235, v235
PW 20,200,220
v_exp_f32 v233, v233
PW 16,180,216
s_waitcnt lgkmcnt(1)
RQ 212,239,38912
PW 16,196,220
s_add_u32 m0, s60, 24576
v_exp_f32 v230, v230
PW 32,180,224
s_waitcnt lgkmcnt(1)
RQ 192,238,36864
CV 216,232,233
PW 32,196,228
RQ 208,238,38912
PW 36,184,224
buffer_load_dwordx4 v237, s[44:47], s56 offen offset:0 lds
PW 36,200,228
v_exp_f32 v250, v250
PW 40,188,224
CV 217,234,235
v_mul_f32 v248, s71, v248
PW 40,204,228
v_exp_f32 v248, v248
PW 44,192,224
s_waitcnt lgkmcnt(1)
CV 220,248,249
v_mul_f32 v229, s71, v229
PW 44,208,228
s_add_u32 m0, s60, 25472
v_exp_f32 v228, v228
QC 232,212,20
s_waitcnt lgkmcnt(0)
v_exp_f32 v229, v229
QK 232,208,16
RT 218,241,30736
QK 232,204,12
buffer_load_dwordx4 v237, s[44:47], s56 offen offset:128 lds
QK 232,200,8
v_exp_f32 v231, v231
QK 232,196,4
RT 220,241,36880
QK 232,192,0
RT 222,241,43024
CV 221,250,251
QC 248,192,24
RT 230,240,44032
QK 248,196,28
CV 224,228,229
QK 248,200,32
buffer_store_dwordx2 a[140:141], v242, s[52:55], s59 offen offset:1664 nt
QK 248,204,36
v_mul_f32 v233, s71, v233
v_mul_f32 v234, s71, v234
QK 248,208,40
CV 225,230,231
v_mul_f32 v235, s71, v235
QK 248,212,44
s_add_u32 m0, s60, 26368
v_exp_f32 v235, v235
QC 228,212,68
RT 224,240,25600
v_mul_f32 v232, s71, v232
QK 228,208,64
v_exp_f32 v234, v234
QK 228,204,60
v_exp_f32 v233, v233
QK 228,200,56
v_exp_f32 v232, v232
QK 228,196,52
RT 226,240,31744
CV 215,234,235
QK 228,192,48
RT 216,241,24592
CV 214,232,233
QC 232,192,72
buffer_load_dwordx4 v237, s[44:47], s56 offen offset:256 lds
QK 232,196,76
RQ 196,239,43008
QK 232,200,80
QK 232,204,84
RT 228,240,37888
QK 232,208,88
v_mul_f32 v251, s71, v251
v_mul_f32 v229, s71, v229
QK 232,212,92
buffer_store_dwordx2 a[142:143], v242, s[52:55], s59 offen offset:2432 nt
v_mul_f32 v248, s71, v248
v_smfmac_f32_16x16x64_bf16 a[232:235], a[240:243], v[204:211], v236
s_waitcnt lgkmcnt(2)
v_exp_f32 v251, v251
PW 60,192,216
v_exp_f32 v229, v229
PW 60,208,220
RQ 204,239,44032
v_mul_f32 v228, s71, v228
PW 56,188,216
s_add_u32 m0, s60, 74624
v_mul_f32 v234, s71, v234
PW 56,204,220
RQ 200,238,44032
v_mul_f32 v230, s71, v230
PW 52,184,216
v_mul_f32 v250, s71, v250
v_mul_f32 v232, s71, v232
PW 52,200,220
RQ 192,238,43008
v_mul_f32 v231, s71, v231
PW 48,180,216
RT 216,240,25616
PW 48,196,220
RT 222,240,44048
v_mul_f32 v249, s71, v249
PW 64,180,224
s_waitcnt lgkmcnt(4)
RT 218,240,31760
v_mul_f32 v233, s71, v233
PW 64,196,228
RT 220,240,37904
PW 68,184,224
v_exp_f32 v232, v232
PW 68,200,228
RQ 208,238,45056
PW 72,188,224
v_exp_f32 v231, v231
PW 72,204,228
RQ 212,239,45056
v_mul_f32 v235, s71, v235
PW 76,192,224
v_exp_f32 v235, v235
PW 76,208,228
s_waitcnt lgkmcnt(2)
v_exp_f32 v249, v249
PW 92,192,216
v_exp_f32 v233, v233
PW 92,208,220
v_exp_f32 v228, v228
PW 88,188,216
s_add_u32 s59, s69, 12288
v_exp_f32 v248, v248
PW 88,204,220
v_exp_f32 v234, v234
PW 84,184,216
s_waitcnt vmcnt(19) lgkmcnt(0)
v_exp_f32 v230, v230
PW 84,200,220
s_barrier
PW 80,180,216
L_s39_%=:
buffer_load_dwordx4 v237, s[48:51], s57 offen offset:128 lds
PW 80,196,220
CV 222,228,229
)"
R"(
CV 223,230,231
QC 228,212,20
v_exp_f32 v250, v250
QK 228,208,16
RT 190,247,1600
QK 228,204,12
s_add_u32 m0, s60, 73728
RT 224,241,37888
CV 227,234,235
QK 228,200,8
RT 188,247,320
QK 228,196,4
s_add_u32 s77, s64, 110592
RT 218,241,44048
CV 226,232,233
QK 228,192,0
RT 220,241,1040
CV 218,248,249
QC 232,192,24
RT 230,241,7168
CV 219,250,251
QK 232,196,28
buffer_load_dwordx4 v237, s[48:51], s57 offen offset:0 lds
QK 232,200,32
RT 226,241,44032
QK 232,204,36
v_mul_f32 v228, s71, v228
v_mul_f32 v230, s71, v230
QK 232,208,40
RT 216,241,37904
v_mul_f32 v231, s71, v231
QK 232,212,44
v_exp_f32 v231, v231
QC 248,212,68
v_exp_f32 v230, v230
QK 248,208,64
v_mul_f32 v229, s71, v229
CV 181,230,231
QK 248,204,60
s_add_u32 m0, s60, 86016
v_exp_f32 v228, v228
QK 248,200,56
buffer_load_dwordx4 v237, s[48:51], s77 offen offset:0 lds
QK 248,196,52
v_exp_f32 v229, v229
QK 248,192,48
RT 228,241,1024
CV 180,228,229
QC 228,192,72
buffer_store_dwordx2 a[144:145], v242, s[52:55], s59 offen offset:128 nt
RQ 192,238,0
QK 228,196,76
RT 222,241,7184
QK 228,200,80
s_add_u32 s56, s64, 129024
v_mul_f32 v232, s71, v232
QK 228,204,84
RQ 204,239,1024
QK 228,208,88
v_mul_f32 v234, s71, v234
v_mul_f32 v250, s71, v250
QK 228,212,92
v_exp_f32 v234, v234
v_smfmac_f32_16x16x64_bf16 a[232:235], a[236:239], v[212:219], v236
s_waitcnt lgkmcnt(2)
v_exp_f32 v232, v232
PW 96,196,224
RQ 200,238,1024
v_mul_f32 v249, s71, v249
PW 96,212,228
v_mul_f32 v251, s71, v251
PW 100,200,224
buffer_store_dwordx2 a[146:147], v242, s[52:55], s59 offen offset:896 nt
v_mul_f32 v235, s71, v235
PW 100,216,228
RQ 208,238,2048
PW 104,204,224
RQ 212,239,2048
v_mul_f32 v230, s71, v230
PW 104,220,228
v_exp_f32 v235, v235
PW 108,208,224
RT 224,240,38912
v_mul_f32 v233, s71, v233
PW 108,224,228
s_add_u32 m0, s60, 36864
RT 226,240,45056
PW 112,208,216
s_waitcnt lgkmcnt(4)
RT 230,240,8192
v_mul_f32 v229, s71, v229
PW 112,224,220
RT 228,240,2048
v_mfma_f32_16x16x32_bf16 a[252:255], v[204:207], a[216:219], a[252:255]
buffer_load_dwordx4 v237, s[44:47], s56 offen offset:0 lds
v_mfma_f32_16x16x32_bf16 a[252:255], v[220:223], a[220:223], a[252:255]
s_add_u32 m0, s60, 37760
RQ 196,239,0
v_mul_f32 v231, s71, v231
v_mfma_f32_16x16x32_bf16 a[248:251], v[200:203], a[216:219], a[248:251]
v_exp_f32 v250, v250
v_mfma_f32_16x16x32_bf16 a[248:251], v[216:219], a[220:223], a[248:251]
v_exp_f32 v251, v251
v_mfma_f32_16x16x32_bf16 a[244:247], v[196:199], a[216:219], a[244:247]
v_exp_f32 v233, v233
v_mfma_f32_16x16x32_bf16 a[244:247], v[212:215], a[220:223], a[244:247]
s_waitcnt lgkmcnt(1)
v_exp_f32 v230, v230
PW 116,196,224
CV 184,232,233
v_mul_f32 v248, s71, v248
PW 116,212,228
v_exp_f32 v249, v249
PW 120,200,224
buffer_load_dwordx4 v237, s[44:47], s56 offen offset:128 lds
PW 120,216,228
RT 216,240,38928
CV 185,234,235
PW 124,204,224
RT 218,240,45072
PW 124,220,228
v_exp_f32 v248, v248
PW 128,208,224
s_add_u32 m0, s60, 38656
v_exp_f32 v231, v231
PW 128,224,228
v_mul_f32 v228, s71, v228
QC 232,212,20
CV 188,248,249
v_lshrrev_b32 v247, 16, v243
QK 232,208,16
CV 193,230,231
QK 232,204,12
buffer_load_dwordx4 v237, s[44:47], s56 offen offset:256 lds
QK 232,200,8
s_waitcnt lgkmcnt(0)
RT 220,240,2064
CV 189,250,251
QK 232,196,4
v_exp_f32 v228, v228
QK 232,192,0
L_c18_%=:
s_cbranch_execz L_c22_%=
RT 222,240,8208
QC 248,192,24
v_exp_f32 v229, v229
QK 248,196,28
RT 226,241,45056
CV 192,228,229
QK 248,200,32
buffer_store_dwordx2 a[148:149], v242, s[52:55], s59 offen offset:1664 nt
QK 248,204,36
s_add_u32 s56, s64, 141312
v_mul_f32 v235, s71, v235
QK 248,208,40
v_exp_f32 v235, v235
QK 248,212,44
v_mul_f32 v233, s71, v233
v_mul_f32 v234, s71, v234
QC 228,212,68
RT 224,241,38912
v_mul_f32 v232, s71, v232
QK 228,208,64
s_add_u32 s57, s64, 122880
v_exp_f32 v233, v233
QK 228,204,60
v_exp_f32 v234, v234
QK 228,200,56
v_exp_f32 v232, v232
QK 228,196,52
RT 228,241,2048
CV 183,234,235
QK 228,192,48
buffer_store_dwordx2 a[150:151], v242, s[52:55], s59 offen offset:2432 nt
CV 182,232,233
QC 232,192,72
QK 232,196,76
QK 232,200,80
QK 232,204,84
v_mul_f32 v248, s71, v248
v_mul_f32 v250, s71, v250
QK 232,208,88
RQ 192,238,6144
QK 232,212,92
RT 230,241,8192
v_smfmac_f32_16x16x64_bf16 a[232:235], a[240:243], v[220:227], v236
RQ 200,238,7168
v_mul_f32 v229, s71, v229
PW 144,208,216
s_waitcnt lgkmcnt(3)
v_mul_f32 v249, s71, v249
PW 144,224,220
RQ 212,239,8192
v_mul_f32 v231, s71, v231
PW 140,204,216
v_exp_f32 v231, v231
PW 140,220,220
RQ 196,239,6144
PW 136,200,216
v_mul_f32 v251, s71, v251
v_mul_f32 v230, s71, v230
PW 136,216,220
RQ 204,239,7168
PW 132,196,216
RT 218,241,45072
PW 132,212,220
RT 222,241,8208
v_mul_f32 v232, s71, v232
PW 148,196,224
s_waitcnt lgkmcnt(4)
RT 216,241,38928
PW 148,212,228
RT 220,241,2064
v_mul_f32 v228, s71, v228
PW 152,200,224
v_mul_f32 v234, s71, v234
v_mul_f32 v235, s71, v235
PW 152,216,228
RQ 208,238,8192
PW 156,204,224
v_exp_f32 v230, v230
PW 156,220,228
v_exp_f32 v229, v229
PW 160,208,224
v_exp_f32 v248, v248
PW 160,224,228
s_waitcnt lgkmcnt(1)
v_mul_f32 v233, s71, v233
PW 176,208,216
v_exp_f32 v232, v232
PW 176,224,220
v_exp_f32 v228, v228
PW 172,204,216
v_exp_f32 v234, v234
PW 172,220,220
s_add_u32 m0, s60, 86912
CV 190,228,229
CV 191,230,231
PW 168,200,216
s_waitcnt vmcnt(19) lgkmcnt(0)
v_exp_f32 v249, v249
PW 168,216,220
s_barrier
PW 164,196,216
L_s40_%=:
buffer_load_dwordx4 v237, s[48:51], s77 offen offset:128 lds
PW 164,212,220
QC 228,212,20
v_exp_f32 v233, v233
QK 228,208,16
v_exp_f32 v235, v235
QK 228,204,12
CV 194,232,233
CV 195,234,235
QK 228,200,8
s_add_u32 s59, s69, 18432
v_exp_f32 v250, v250
QK 228,196,4
s_add_u32 m0, s60, 87808
RT 222,240,18448
QK 228,192,0
RT 224,240,0
CV 186,248,249
QC 232,192,24
v_exp_f32 v251, v251
QK 232,196,28
RT 226,240,6144
CV 187,250,251
QK 232,200,32
buffer_load_dwordx4 v237, s[48:51], s77 offen offset:256 lds
QK 232,204,36
RT 230,240,18432
v_mul_f32 v230, s71, v230
QK 232,208,40
s_add_u32 m0, s60, 50944
v_mul_f32 v228, s71, v228
v_mul_f32 v231, s71, v231
QK 232,212,44
v_exp_f32 v228, v228
QC 248,212,68
v_exp_f32 v231, v231
QK 248,208,64
v_exp_f32 v230, v230
QK 248,204,60
v_mul_f32 v229, s71, v229
CV 197,230,231
QK 248,200,56
v_exp_f32 v229, v229
QK 248,196,52
buffer_load_dwordx4 v237, s[48:51], s57 offen offset:256 lds
QK 248,192,48
RT 218,240,6160
CV 196,228,229
QC 228,192,72
RT 228,240,12288
QK 228,196,76
RT 216,240,16
QK 228,200,80
QK 228,204,84
RQ 196,239,12288
QK 228,208,88
v_mul_f32 v232, s71, v232
v_mul_f32 v233, s71, v233
QK 228,212,92
v_mul_f32 v249, s71, v249
v_smfmac_f32_16x16x64_bf16 a[232:235], a[236:239], v[180:187], v236
s_waitcnt lgkmcnt(2)
RT 220,240,12304
v_mul_f32 v248, s71, v248
PW 0,212,224
RQ 204,239,13312
PW 0,180,228
buffer_store_dwordx2 a[152:153], v242, s[52:55], s59 offen offset:128 nt
RQ 200,238,13312
PW 4,216,224
RQ 192,238,12288
PW 4,184,228
v_exp_f32 v232, v232
PW 8,220,224
buffer_store_dwordx2 a[154:155], v242, s[52:55], s59 offen offset:896 nt
v_mul_f32 v251, s71, v251
PW 8,188,228
RQ 208,238,14336
PW 12,224,224
RT 226,241,6144
v_mul_f32 v250, s71, v250
PW 12,192,228
s_waitcnt lgkmcnt(3)
RT 224,241,0
v_mul_f32 v234, s71, v234
PW 28,224,216
RT 230,241,18432
PW 28,192,220
s_add_u32 m0, s60, 0
RT 228,241,12288
PW 24,220,216
v_exp_f32 v250, v250
PW 24,188,220
v_exp_f32 v251, v251
PW 20,216,216
RQ 212,239,14336
v_mul_f32 v228, s71, v228
PW 20,184,220
v_exp_f32 v228, v228
PW 16,212,216
v_mul_f32 v229, s71, v229
v_mul_f32 v231, s71, v231
PW 16,180,220
s_waitcnt lgkmcnt(1)
v_exp_f32 v248, v248
PW 32,212,224
v_mul_f32 v235, s71, v235
v_mul_f32 v230, s71, v230
PW 32,180,228
v_exp_f32 v233, v233
PW 36,216,224
v_exp_f32 v234, v234
PW 36,184,228
v_exp_f32 v235, v235
PW 40,220,224
v_exp_f32 v229, v229
PW 40,188,228
CV 200,232,233
PW 44,224,224
RT 216,241,16
CV 201,234,235
PW 44,192,228
s_waitcnt lgkmcnt(1)
CV 208,228,229
QC 232,212,20
buffer_load_dwordx4 v237, s[44:47], s56 offen offset:0 lds
QK 232,208,16
s_add_u32 m0, s60, 896
RT 222,241,18448
CV 205,250,251
QK 232,204,12
v_exp_f32 v231, v231
QK 232,200,8
v_exp_f32 v230, v230
QK 232,196,4
v_exp_f32 v249, v249
QK 232,192,0
RT 224,240,1024
CV 204,248,249
QC 248,192,24
RT 218,241,6160
QK 248,196,28
QK 248,200,32
buffer_store_dwordx2 a[156:157], v242, s[52:55], s59 offen offset:1664 nt
QK 248,204,36
CV 209,230,231
v_mul_f32 v235, s71, v235
QK 248,208,40
RT 230,240,19456
v_mul_f32 v234, s71, v234
QK 248,212,44
v_mul_f32 v232, s71, v232
v_mul_f32 v233, s71, v233
QC 228,212,68
v_exp_f32 v233, v233
)"
R"(
QK 228,208,64
v_exp_f32 v232, v232
QK 228,204,60
v_exp_f32 v234, v234
QK 228,200,56
v_exp_f32 v235, v235
QK 228,196,52
CV 198,232,233
v_mul_f32 v249, s71, v249
QK 228,192,48
RT 220,241,12304
CV 199,234,235
QC 232,192,72
buffer_load_dwordx4 v237, s[44:47], s56 offen offset:128 lds
QK 232,196,76
s_add_u32 m0, s60, 1792
v_mul_f32 v251, s71, v251
QK 232,200,80
RT 226,240,7168
QK 232,204,84
v_mul_f32 v228, s71, v228
v_mul_f32 v231, s71, v231
QK 232,208,88
RQ 208,238,20480
QK 232,212,92
RT 228,240,13312
v_smfmac_f32_16x16x64_bf16 a[232:235], a[240:243], v[188:195], v236
s_waitcnt lgkmcnt(3)
RQ 204,239,19456
PW 60,224,216
RQ 212,239,20480
PW 60,192,220
RQ 196,239,18432
PW 56,220,216
RQ 200,238,19456
v_mul_f32 v230, s71, v230
PW 56,188,220
buffer_store_dwordx2 a[158:159], v242, s[52:55], s59 offen offset:2432 nt
RQ 192,238,18432
PW 52,216,216
v_mul_f32 v250, s71, v250
PW 52,184,220
v_exp_f32 v249, v249
PW 48,212,216
s_add_u32 s77, s64, 135168
RT 218,240,7184
v_mul_f32 v234, s71, v234
PW 48,180,220
s_waitcnt lgkmcnt(3)
RT 216,240,1040
v_mul_f32 v229, s71, v229
PW 64,212,224
RT 220,240,13328
v_mul_f32 v233, s71, v233
PW 64,180,228
RT 222,240,19472
v_mul_f32 v232, s71, v232
PW 68,216,224
s_add_u32 s59, s69, 0
v_exp_f32 v232, v232
PW 68,184,228
v_exp_f32 v230, v230
PW 72,220,224
v_exp_f32 v233, v233
PW 72,188,228
buffer_load_dwordx4 v237, s[44:47], s56 offen offset:256 lds
PW 76,224,224
v_exp_f32 v229, v229
PW 76,192,228
s_waitcnt lgkmcnt(0)
v_exp_f32 v228, v228
PW 92,224,216
v_exp_f32 v231, v231
PW 92,192,220
CV 206,228,229
PW 88,220,216
v_exp_f32 v251, v251
PW 88,188,220
s_add_u32 m0, s60, 50048
CV 207,230,231
PW 84,216,216
s_waitcnt vmcnt(19)
v_mul_f32 v235, s71, v235
PW 84,184,220
s_barrier
PW 80,212,216
L_s41_%=:
buffer_load_dwordx4 v237, s[48:51], s57 offen offset:128 lds
PW 80,180,220
s_add_u32 m0, s60, 49152
v_exp_f32 v235, v235
QC 228,212,20
QK 228,208,16
v_exp_f32 v234, v234
QK 228,204,12
v_exp_f32 v250, v250
QK 228,200,8
CV 210,232,233
QK 228,196,4
RT 224,241,13312
CV 203,250,251
QK 228,192,0
v_mul_f32 v248, s71, v248
CV 211,234,235
QC 232,192,24
buffer_load_dwordx4 v237, s[48:51], s57 offen offset:0 lds
QK 232,196,28
RT 230,241,31744
QK 232,200,32
v_exp_f32 v248, v248
QK 232,204,36
v_mul_f32 v228, s71, v228
v_mul_f32 v230, s71, v230
QK 232,208,40
RT 226,241,19456
v_mul_f32 v231, s71, v231
QK 232,212,44
CV 202,248,249
v_mul_f32 v229, s71, v229
QC 248,212,68
s_add_u32 m0, s60, 61440
v_exp_f32 v231, v231
QK 248,208,64
v_exp_f32 v229, v229
QK 248,204,60
buffer_load_dwordx4 v237, s[48:51], s77 offen offset:0 lds
QK 248,200,56
v_exp_f32 v230, v230
QK 248,196,52
v_exp_f32 v228, v228
QK 248,192,48
CV 212,228,229
CV 213,230,231
QC 228,192,72
RT 228,241,25600
QK 228,196,76
RT 218,241,19472
QK 228,200,80
RQ 192,238,24576
v_mul_f32 v232, s71, v232
QK 228,204,84
buffer_store_dwordx2 a[160:161], v242, s[52:55], s59 offen offset:256 nt
RT 216,241,13328
QK 228,208,88
RQ 196,239,24576
v_mul_f32 v248, s71, v248
QK 228,212,92
RT 220,241,25616
v_smfmac_f32_16x16x64_bf16 a[232:235], a[236:239], v[196:203], v236
s_waitcnt lgkmcnt(5)
buffer_store_dwordx2 a[162:163], v242, s[52:55], s59 offen offset:1024 nt
v_mul_f32 v235, s71, v235
PW 96,180,224
v_mul_f32 v233, s71, v233
v_mul_f32 v249, s71, v249
PW 96,196,228
RT 222,241,31760
PW 100,184,224
v_exp_f32 v232, v232
PW 100,200,228
RQ 200,238,25600
v_mul_f32 v229, s71, v229
PW 104,188,224
RQ 208,238,26624
v_mul_f32 v234, s71, v234
PW 104,204,228
v_exp_f32 v234, v234
PW 108,192,224
RT 226,240,20480
PW 108,208,228
s_waitcnt lgkmcnt(3)
RT 224,240,14336
v_mul_f32 v250, s71, v250
PW 112,192,216
RT 228,240,26624
PW 112,208,220
RT 230,240,32768
v_mul_f32 v228, s71, v228
v_mfma_f32_16x16x32_bf16 a[252:255], v[188:191], a[216:219], a[252:255]
s_add_u32 s56, s64, 153600
v_exp_f32 v249, v249
v_mfma_f32_16x16x32_bf16 a[252:255], v[204:207], a[220:223], a[252:255]
RQ 212,239,26624
v_mfma_f32_16x16x32_bf16 a[248:251], v[184:187], a[216:219], a[248:251]
v_mul_f32 v251, s71, v251
v_mfma_f32_16x16x32_bf16 a[248:251], v[200:203], a[220:223], a[248:251]
v_exp_f32 v235, v235
v_mfma_f32_16x16x32_bf16 a[244:247], v[180:183], a[216:219], a[244:247]
v_exp_f32 v233, v233
v_mfma_f32_16x16x32_bf16 a[244:247], v[196:199], a[220:223], a[244:247]
s_waitcnt lgkmcnt(1)
CV 216,232,233
PW 116,180,224
v_exp_f32 v248, v248
PW 116,196,228
v_mul_f32 v231, s71, v231
PW 120,184,224
RQ 204,239,25600
CV 217,234,235
PW 120,200,228
s_add_u32 m0, s60, 12288
v_exp_f32 v251, v251
PW 124,188,224
v_exp_f32 v228, v228
PW 124,204,228
v_exp_f32 v231, v231
PW 128,192,224
v_exp_f32 v250, v250
PW 128,208,228
s_waitcnt lgkmcnt(1)
RT 216,240,14352
QC 232,212,20
v_mul_f32 v230, s71, v230
QK 232,208,16
s_waitcnt lgkmcnt(1)
RT 220,240,26640
CV 221,250,251
QK 232,204,12
v_exp_f32 v230, v230
QK 232,200,8
buffer_load_dwordx4 v237, s[44:47], s56 offen offset:0 lds
QK 232,196,4
v_exp_f32 v229, v229
QK 232,192,0
RT 222,240,32784
CV 220,248,249
QC 248,192,24
RT 218,240,20496
CV 224,228,229
QK 248,196,28
RT 226,241,20480
QK 248,200,32
CV 225,230,231
QK 248,204,36
s_add_u32 m0, s60, 13184
v_mul_f32 v232, s71, v232
v_mul_f32 v235, s71, v235
QK 248,208,40
v_mul_f32 v233, s71, v233
v_mul_f32 v234, s71, v234
QK 248,212,44
buffer_load_dwordx4 v237, s[44:47], s56 offen offset:128 lds
QC 228,212,68
RT 230,241,32768
QK 228,208,64
v_exp_f32 v235, v235
QK 228,204,60
v_exp_f32 v234, v234
QK 228,200,56
v_exp_f32 v233, v233
QK 228,196,52
v_exp_f32 v232, v232
QK 228,192,48
CV 214,232,233
CV 215,234,235
QC 232,192,72
RT 228,241,26624
QK 232,196,76
RT 224,241,14336
QK 232,200,80
buffer_store_dwordx2 a[164:165], v242, s[52:55], s59 offen offset:1792 nt
RQ 200,238,31744
QK 232,204,84
RQ 204,239,31744
v_mul_f32 v248, s71, v248
QK 232,208,88
s_add_u32 m0, s60, 14080
RQ 192,238,30720
v_mul_f32 v251, s71, v251
QK 232,212,92
RQ 208,238,32768
v_mul_f32 v229, s71, v229
v_smfmac_f32_16x16x64_bf16 a[232:235], a[240:243], v[204:211], v236
s_waitcnt lgkmcnt(5)
RQ 212,239,32768
v_mul_f32 v250, s71, v250
PW 144,192,216
v_mul_f32 v230, s71, v230
PW 144,208,220
v_exp_f32 v230, v230
PW 140,188,216
buffer_store_dwordx2 a[166:167], v242, s[52:55], s59 offen offset:2560 nt
v_mul_f32 v231, s71, v231
PW 140,204,220
v_exp_f32 v229, v229
PW 136,184,216
v_exp_f32 v231, v231
PW 136,200,220
v_exp_f32 v251, v251
PW 132,180,216
RT 216,241,14352
PW 132,196,220
s_waitcnt lgkmcnt(1)
RT 222,241,32784
v_add_u32 v244, 135168, v238
PW 148,180,224
RT 218,241,20496
v_mul_f32 v228, s71, v228
PW 148,196,228
RT 220,241,26640
v_mul_f32 v234, s71, v234
PW 152,184,224
RQ 196,239,30720
v_mul_f32 v249, s71, v249
PW 152,200,228
v_exp_f32 v234, v234
PW 156,188,224
buffer_load_dwordx4 v237, s[44:47], s56 offen offset:256 lds
PW 156,204,228
s_add_u32 s57, s64, 172032
v_exp_f32 v228, v228
PW 160,192,224
s_add_u32 s59, s69, 6144
v_mul_f32 v232, s71, v232
PW 160,208,228
s_waitcnt lgkmcnt(1)
v_exp_f32 v250, v250
PW 176,192,216
s_add_u32 m0, s60, 62336
CV 223,230,231
PW 176,208,220
CV 222,228,229
v_mul_f32 v235, s71, v235
PW 172,188,216
v_exp_f32 v248, v248
PW 172,204,220
v_exp_f32 v249, v249
PW 168,184,216
s_waitcnt vmcnt(19) lgkmcnt(0)
v_exp_f32 v235, v235
PW 168,200,220
s_barrier
PW 164,180,216
L_s42_%=:
buffer_load_dwordx4 v237, s[48:51], s77 offen offset:128 lds
PW 164,196,220
RT 218,240,30736
v_mul_f32 v233, s71, v233
QC 228,212,20
s_add_u32 m0, s60, 63232
v_exp_f32 v233, v233
QK 228,208,16
RT 222,240,43024
QK 228,204,12
RT 226,240,30720
QK 228,200,8
CV 219,250,251
CV 227,234,235
QK 228,196,4
v_exp_f32 v232, v232
QK 228,192,0
L_c19_%=:
s_cbranch_execz L_ret2_%=
RT 224,240,24576
CV 226,232,233
QC 232,192,24
buffer_load_dwordx4 v237, s[48:51], s77 offen offset:256 lds
QK 232,196,28
CV 218,248,249
QK 232,200,32
s_mov_b32 s56, s67
v_add_u32 v247, s61, v247
QK 232,204,36
RT 220,240,36880
v_mul_f32 v228, s71, v228
QK 232,208,40
v_mul_f32 v229, s71, v229
v_mul_f32 v230, s71, v230
QK 232,212,44
v_exp_f32 v229, v229
QC 248,212,68
RT 230,240,43008
v_mul_f32 v231, s71, v231
QK 248,208,64
s_add_u32 m0, s60, 75520
v_exp_f32 v230, v230
QK 248,204,60
buffer_load_dwordx4 v237, s[48:51], s57 offen offset:256 lds
QK 248,200,56
v_exp_f32 v228, v228
QK 248,196,52
v_exp_f32 v231, v231
QK 248,192,48
CV 180,228,229
CV 181,230,231
QC 228,192,72
RT 228,240,36864
QK 228,196,76
s_add_u32 m0, s60, 24576
v_mul_f32 v234, s71, v234
v_mul_f32 v235, s71, v235
QK 228,200,80
buffer_store_dwordx2 a[168:169], v242, s[52:55], s59 offen offset:256 nt
QK 228,204,84
RQ 196,239,36864
v_mul_f32 v249, s71, v249
QK 228,208,88
buffer_load_dwordx4 v237, s[44:47], s56 offen offset:0 lds
QK 228,212,92
RT 216,240,24592
)"
R"(
v_mul_f32 v232, s71, v232
v_smfmac_f32_16x16x64_bf16 a[232:235], a[236:239], v[212:219], v236
s_waitcnt lgkmcnt(2)
RQ 208,238,38912
PW 0,196,224
RQ 212,239,38912
v_mul_f32 v250, s71, v250
PW 0,212,228
RQ 192,238,36864
PW 4,200,224
v_exp_f32 v234, v234
PW 4,216,228
v_exp_f32 v249, v249
PW 8,204,224
buffer_store_dwordx2 a[170:171], v242, s[52:55], s59 offen offset:1024 nt
RQ 204,239,37888
PW 8,220,228
v_mul_f32 v233, s71, v233
PW 12,208,224
RT 226,241,30720
v_mul_f32 v251, s71, v251
PW 12,224,228
s_waitcnt lgkmcnt(2)
s_add_u32 m0, s60, 25472
RT 224,241,24576
PW 28,208,216
RT 230,241,43008
v_mul_f32 v228, s71, v228
PW 28,224,220
RT 228,241,36864
v_mul_f32 v231, s71, v231
PW 24,204,216
buffer_load_dwordx4 v237, s[44:47], s56 offen offset:128 lds
PW 24,220,220
v_exp_f32 v233, v233
PW 20,200,216
v_exp_f32 v228, v228
PW 20,216,220
v_exp_f32 v232, v232
PW 16,196,216
v_mul_f32 v248, s71, v248
PW 16,212,220
s_waitcnt lgkmcnt(0)
v_exp_f32 v251, v251
PW 32,196,224
RQ 200,238,37888
v_mul_f32 v230, s71, v230
PW 32,212,228
v_exp_f32 v250, v250
PW 36,200,224
v_exp_f32 v235, v235
PW 36,216,228
s_add_u32 m0, s60, 26368
RT 218,241,30736
CV 185,234,235
PW 40,204,224
buffer_load_dwordx4 v237, s[44:47], s56 offen offset:256 lds
PW 40,220,228
v_mul_f32 v229, s71, v229
PW 44,208,224
PW 44,224,228
CV 184,232,233
QC 232,212,20
RT 222,241,43024
CV 189,250,251
QK 232,208,16
v_exp_f32 v230, v230
QK 232,204,12
s_waitcnt lgkmcnt(1)
QK 232,200,8
v_exp_f32 v231, v231
QK 232,196,4
v_exp_f32 v248, v248
QK 232,192,0
CV 188,248,249
QC 248,192,24
buffer_store_dwordx2 a[172:173], v242, s[52:55], s59 offen offset:1792 nt
QK 248,196,28
v_exp_f32 v229, v229
QK 248,200,32
CV 192,228,229
CV 193,230,231
QK 248,204,36
RT 216,241,24592
v_mul_f32 v232, s71, v232
QK 248,208,40
RT 220,241,36880
v_mul_f32 v233, s71, v233
QK 248,212,44
s_add_u32 m0, s60, 73728
v_mul_f32 v234, s71, v234
v_mul_f32 v235, s71, v235
QC 228,212,68
v_exp_f32 v235, v235
QK 228,208,64
buffer_store_dwordx2 a[174:175], v242, s[52:55], s59 offen offset:2560 nt
RT 228,240,37888
QK 228,204,60
v_exp_f32 v232, v232
QK 228,200,56
v_exp_f32 v233, v233
QK 228,196,52
v_exp_f32 v234, v234
QK 228,192,48
CV 182,232,233
CV 183,234,235
QC 232,192,72
RT 226,240,31744
QK 232,196,76
v_mul_f32 v250, s71, v250
QK 232,200,80
v_mul_f32 v251, s71, v251
QK 232,204,84
RT 230,240,44032
v_mul_f32 v248, s71, v248
QK 232,208,88
RQ 196,239,43008
QK 232,212,92
v_smfmac_f32_16x16x64_bf16 a[232:235], a[240:243], v[220:227], v236
s_waitcnt lgkmcnt(2)
RQ 208,238,45056
v_mul_f32 v229, s71, v229
PW 60,208,216
RT 224,240,25600
PW 60,224,220
RQ 200,238,44032
v_mul_f32 v231, s71, v231
PW 56,204,216
RQ 192,238,43008
PW 56,220,220
v_mul_f32 v232, s71, v232
PW 52,200,216
v_exp_f32 v248, v248
PW 52,216,220
RQ 212,239,45056
v_mul_f32 v235, s71, v235
PW 48,196,216
RT 218,240,31760
v_mul_f32 v233, s71, v233
PW 48,212,220
s_waitcnt lgkmcnt(3)
RT 216,240,25616
PW 64,196,224
RT 222,240,44048
PW 64,212,228
RT 220,240,37904
v_mul_f32 v234, s71, v234
PW 68,200,224
s_add_u32 s59, s69, 12288
v_exp_f32 v250, v250
PW 68,216,228
RQ 204,239,44032
v_mul_f32 v228, s71, v228
PW 72,204,224
v_exp_f32 v231, v231
PW 72,220,228
v_exp_f32 v233, v233
PW 76,208,224
v_exp_f32 v229, v229
PW 76,224,228
s_waitcnt lgkmcnt(1)
v_exp_f32 v228, v228
PW 92,208,216
v_exp_f32 v235, v235
PW 92,224,220
v_mul_f32 v230, s71, v230
PW 88,204,216
v_exp_f32 v230, v230
PW 88,220,220
v_exp_f32 v234, v234
PW 84,200,216
s_waitcnt vmcnt(19) lgkmcnt(0)
CV 190,228,229
CV 191,230,231
PW 84,216,220
s_barrier
PW 80,196,216
L_s43_%=:
buffer_load_dwordx4 v237, s[48:51], s57 offen offset:0 lds
PW 80,212,220
v_exp_f32 v232, v232
QC 228,212,20
v_exp_f32 v251, v251
QK 228,208,16
RT 230,241,7168
CV 187,250,251
QK 228,204,12
RT 228,241,1024
QK 228,200,8
s_add_u32 m0, s60, 74624
v_mul_f32 v249, s71, v249
CV 194,232,233
QK 228,196,4
RT 226,241,44032
QK 228,192,0
CV 195,234,235
QC 232,192,24
buffer_load_dwordx4 v237, s[48:51], s57 offen offset:128 lds
QK 232,196,28
s_add_u32 s77, s64, 184320
v_exp_f32 v249, v249
QK 232,200,32
RT 218,241,44048
CV 186,248,249
QK 232,204,36
v_mul_f32 v228, s71, v228
v_mul_f32 v230, s71, v230
QK 232,208,40
v_exp_f32 v228, v228
QK 232,212,44
s_add_u32 m0, s60, 86016
v_exp_f32 v230, v230
QC 248,212,68
v_mul_f32 v229, s71, v229
v_mul_f32 v231, s71, v231
QK 248,208,64
v_exp_f32 v231, v231
QK 248,204,60
buffer_load_dwordx4 v237, s[48:51], s77 offen offset:0 lds
QK 248,200,56
v_exp_f32 v229, v229
QK 248,196,52
RT 216,241,37904
CV 197,230,231
QK 248,192,48
RT 224,241,37888
CV 196,228,229
QC 228,192,72
QK 228,196,76
RQ 196,239,0
QK 228,200,80
buffer_store_dwordx2 a[176:177], v242, s[52:55], s59 offen offset:256 nt
RT 222,241,7184
QK 228,204,84
RT 220,241,1040
v_mul_f32 v233, s71, v233
QK 228,208,88
buffer_store_dwordx2 a[178:179], v242, s[52:55], s59 offen offset:1024 nt
v_mul_f32 v232, s71, v232
QK 228,212,92
RQ 192,238,0
v_mul_f32 v249, s71, v249
v_smfmac_f32_16x16x64_bf16 a[232:235], a[236:239], v[180:187], v236
s_waitcnt lgkmcnt(3)
PW 96,212,224
RQ 204,239,1024
v_mul_f32 v235, s71, v235
PW 96,180,228
v_exp_f32 v233, v233
PW 100,216,224
v_mul_f32 v248, s71, v248
PW 100,184,228
v_mul_f32 v230, s71, v230
PW 104,220,224
v_exp_f32 v232, v232
PW 104,188,228
RQ 212,239,2048
PW 108,224,224
RT 224,240,38912
PW 108,192,228
RT 228,240,2048
v_mul_f32 v234, s71, v234
PW 112,224,216
s_waitcnt lgkmcnt(3)
RT 226,240,45056
v_mul_f32 v251, s71, v251
PW 112,192,220
RT 230,240,8192
v_mul_f32 v229, s71, v229
v_mfma_f32_16x16x32_bf16 a[252:255], v[220:223], a[216:219], a[252:255]
v_exp_f32 v248, v248
v_mfma_f32_16x16x32_bf16 a[252:255], v[188:191], a[220:223], a[252:255]
v_exp_f32 v235, v235
v_mfma_f32_16x16x32_bf16 a[248:251], v[216:219], a[216:219], a[248:251]
v_exp_f32 v251, v251
v_mfma_f32_16x16x32_bf16 a[248:251], v[184:187], a[220:223], a[248:251]
v_mfma_f32_16x16x32_bf16 a[244:247], v[212:215], a[216:219], a[244:247]
RQ 208,238,2048
v_mul_f32 v231, s71, v231
v_mfma_f32_16x16x32_bf16 a[244:247], v[180:183], a[220:223], a[244:247]
s_waitcnt lgkmcnt(1)
v_exp_f32 v234, v234
PW 116,212,224
v_exp_f32 v249, v249
PW 116,180,228
RQ 200,238,1024
CV 201,234,235
PW 120,216,224
v_exp_f32 v231, v231
PW 120,184,228
s_add_u32 m0, s60, 86912
v_mul_f32 v250, s71, v250
CV 204,248,249
PW 124,220,224
v_exp_f32 v250, v250
PW 124,188,228
RT 216,240,38928
CV 200,232,233
PW 128,224,224
RT 222,240,8208
v_mul_f32 v228, s71, v228
PW 128,192,228
v_exp_f32 v228, v228
QC 232,212,20
s_waitcnt lgkmcnt(2)
v_exp_f32 v230, v230
QK 232,208,16
CV 205,250,251
QK 232,204,12
v_exp_f32 v229, v229
QK 232,200,8
QK 232,196,4
RT 220,240,2064
QK 232,192,0
CV 209,230,231
QC 248,192,24
CV 208,228,229
QK 248,196,28
RT 226,241,45056
QK 248,200,32
RT 224,241,38912
QK 248,204,36
v_mul_f32 v233, s71, v233
v_mul_f32 v234, s71, v234
QK 248,208,40
RT 228,241,2048
v_mul_f32 v235, s71, v235
QK 248,212,44
v_exp_f32 v233, v233
QC 228,212,68
v_mul_f32 v232, s71, v232
QK 228,208,64
v_exp_f32 v235, v235
QK 228,204,60
v_exp_f32 v232, v232
QK 228,200,56
v_exp_f32 v234, v234
QK 228,196,52
CV 198,232,233
CV 199,234,235
QK 228,192,48
RT 218,240,45072
QC 232,192,72
buffer_store_dwordx2 a[180:181], v242, s[52:55], s59 offen offset:1792 nt
QK 232,196,76
QK 232,200,80
RT 230,241,8192
v_mul_f32 v248, s71, v248
QK 232,204,84
buffer_store_dwordx2 a[182:183], v242, s[52:55], s59 offen offset:2560 nt
QK 232,208,88
v_mul_f32 v250, s71, v250
v_mul_f32 v251, s71, v251
QK 232,212,92
v_exp_f32 v248, v248
v_smfmac_f32_16x16x64_bf16 a[232:235], a[240:243], v[188:195], v236
s_waitcnt lgkmcnt(1)
RQ 196,239,6144
v_mul_f32 v231, s71, v231
PW 144,224,216
RQ 208,238,8192
v_mul_f32 v228, s71, v228
PW 144,192,220
RQ 192,238,6144
PW 140,220,216
v_exp_f32 v231, v231
PW 140,188,220
RQ 212,239,8192
v_mul_f32 v230, s71, v230
PW 136,216,216
v_exp_f32 v230, v230
PW 136,184,220
RQ 204,239,7168
v_mul_f32 v249, s71, v249
PW 132,212,216
s_add_u32 s59, s69, 18432
RT 218,241,45072
v_mul_f32 v235, s71, v235
PW 132,180,220
RT 222,241,8208
v_mul_f32 v234, s71, v234
PW 148,212,224
s_waitcnt lgkmcnt(4)
RT 216,241,38928
v_mul_f32 v233, s71, v233
PW 148,180,228
RT 220,241,2064
v_mul_f32 v232, s71, v232
PW 152,216,224
v_exp_f32 v233, v233
PW 152,184,228
v_mul_f32 v229, s71, v229
CV 207,230,231
PW 156,220,224
v_exp_f32 v229, v229
PW 156,188,228
RQ 200,238,7168
PW 160,224,224
v_exp_f32 v235, v235
PW 160,192,228
)"
R"(
s_waitcnt lgkmcnt(1)
v_exp_f32 v249, v249
PW 176,224,216
v_exp_f32 v232, v232
PW 176,192,220
v_exp_f32 v251, v251
PW 172,220,216
CV 210,232,233
PW 172,188,220
v_exp_f32 v228, v228
PW 168,216,216
s_waitcnt vmcnt(16) lgkmcnt(0)
v_exp_f32 v250, v250
PW 168,184,220
s_barrier
PW 164,212,216
L_s44_%=:
buffer_load_dwordx4 v237, s[48:51], s77 offen offset:128 lds
PW 164,180,220
CV 203,250,251
CV 206,228,229
QC 228,212,20
v_exp_f32 v234, v234
QK 228,208,16
s_add_u32 m0, s60, 87808
RT 226,240,6144
QK 228,204,12
RT 228,240,12288
CV 211,234,235
QK 228,200,8
RT 216,240,16
QK 228,196,4
RT 224,240,0
v_add_u32 v245, 135168, v239
QK 228,192,0
RT 230,240,18432
CV 202,248,249
QC 232,192,24
buffer_load_dwordx4 v237, s[48:51], s77 offen offset:256 lds
QK 232,196,28
RT 218,240,6160
QK 232,200,32
RT 220,240,12304
QK 232,204,36
RT 222,240,18448
v_mul_f32 v231, s71, v231
QK 232,208,40
v_mul_f32 v228, s71, v228
v_mul_f32 v229, s71, v229
QK 232,212,44
v_exp_f32 v228, v228
v_smfmac_f32_16x16x64_bf16 a[232:235], a[236:239], v[196:203], v236
s_waitcnt lgkmcnt(3)
PW 0,180,224
buffer_store_dwordx2 a[184:185], v242, s[52:55], s59 offen offset:256 nt
v_mul_f32 v230, s71, v230
PW 0,196,228
v_exp_f32 v231, v231
PW 4,184,224
PW 4,200,228
v_exp_f32 v229, v229
PW 8,188,224
CV 212,228,229
PW 8,204,228
v_exp_f32 v230, v230
PW 12,192,224
v_mul_f32 v233, s71, v233
v_mul_f32 v235, s71, v235
PW 12,208,228
s_waitcnt lgkmcnt(0)
buffer_store_dwordx2 a[186:187], v242, s[52:55], s59 offen offset:1024 nt
v_mul_f32 v234, s71, v234
PW 28,192,216
RT 226,241,6144
CV 213,230,231
PW 28,208,220
v_exp_f32 v233, v233
PW 24,188,216
v_mul_f32 v232, s71, v232
PW 24,204,220
v_exp_f32 v235, v235
QC 228,212,68
v_exp_f32 v232, v232
QK 228,208,64
RT 230,241,18432
QK 228,204,60
QK 228,200,56
RT 224,241,0
QK 228,196,52
v_exp_f32 v234, v234
QK 228,192,48
CV 216,232,233
CV 217,234,235
QC 232,192,72
RQ 192,238,12288
QK 232,196,76
QK 232,200,80
RT 228,241,12288
QK 232,204,84
RQ 204,239,13312
v_mul_f32 v229, s71, v229
QK 232,208,88
v_mul_f32 v228, s71, v228
v_mul_f32 v230, s71, v230
QK 232,212,92
PW 20,184,216
RQ 196,239,12288
v_mul_f32 v231, s71, v231
PW 20,200,220
v_exp_f32 v231, v231
PW 16,180,216
v_exp_f32 v228, v228
PW 16,196,220
s_waitcnt lgkmcnt(1)
RQ 208,238,14336
v_mul_f32 v232, s71, v232
PW 32,180,224
RQ 212,239,14336
v_accvgpr_write_b32 a97, v213
PW 32,196,228
RT 218,241,6160
v_mul_f32 v235, s71, v235
PW 36,184,224
RQ 200,238,13312
v_mul_f32 v233, s71, v233
PW 36,200,228
v_exp_f32 v229, v229
PW 40,188,224
v_exp_f32 v230, v230
PW 40,204,228
v_exp_f32 v235, v235
PW 44,192,224
CV 220,228,229
CV 221,230,231
PW 44,208,228
s_waitcnt lgkmcnt(1)
RT 222,241,18448
QC 228,212,20
v_exp_f32 v232, v232
QK 228,208,16
RT 216,241,16
v_mul_f32 v234, s71, v234
QK 228,204,12
s_waitcnt lgkmcnt(2)
v_exp_f32 v234, v234
QK 228,200,8
v_exp_f32 v233, v233
QK 228,196,4
RT 224,240,1024
CV 225,234,235
QK 228,192,0
RT 220,241,12304
CV 224,232,233
QC 232,192,24
v_accvgpr_write_b32 a104, v220
QK 232,196,28
QK 232,200,32
RT 226,240,7168
QK 232,204,36
L_c20_%=:
s_cbranch_execz L_ret2_%=
v_mul_f32 v228, s71, v228
QK 232,208,40
v_mul_f32 v229, s71, v229
v_mul_f32 v231, s71, v231
QK 232,212,44
v_accvgpr_write_b32 a101, v217
v_mul_f32 v230, s71, v230
v_smfmac_f32_16x16x64_bf16 a[232:235], a[240:243], v[204:211], v236
s_waitcnt lgkmcnt(1)
v_exp_f32 v230, v230
PW 60,192,216
RT 228,240,13312
PW 60,208,220
RT 230,240,19456
PW 56,188,216
v_exp_f32 v231, v231
PW 56,204,220
v_mul_f32 v233, s71, v233
v_mul_f32 v234, s71, v234
PW 52,184,216
v_mul_f32 v232, s71, v232
PW 52,200,220
v_exp_f32 v228, v228
PW 48,180,216
buffer_store_dwordx2 a[188:189], v242, s[52:55], s59 offen offset:1792 nt
RT 218,240,7184
PW 48,196,220
s_waitcnt lgkmcnt(1)
v_accvgpr_write_b32 a100, v216
CV 215,230,231
PW 64,180,224
v_exp_f32 v229, v229
PW 64,196,228
v_mul_f32 v235, s71, v235
PW 68,184,224
v_exp_f32 v233, v233
PW 68,200,228
buffer_store_dwordx2 a[190:191], v242, s[52:55], s59 offen offset:2560 nt
CV 214,228,229
QC 228,212,68
v_exp_f32 v234, v234
QK 228,208,64
RT 216,240,1040
v_and_b32 v242, 0xffff, v243
QK 228,204,60
v_exp_f32 v232, v232
QK 228,200,56
v_exp_f32 v235, v235
QK 228,196,52
RT 220,240,13328
CV 219,234,235
QK 228,192,48
RT 222,240,19472
CV 218,232,233
QC 232,192,72
RQ 192,238,18432
v_add_u32 v243, 24576, v240
QK 232,196,76
RQ 196,239,18432
v_accvgpr_write_b32 a103, v219
QK 232,200,80
RQ 200,238,19456
v_accvgpr_write_b32 a105, v221
QK 232,204,84
v_mul_f32 v231, s71, v231
v_add_u32 v237, 24576, v241
QK 232,208,88
RQ 204,239,19456
v_accvgpr_write_b32 a98, v214
QK 232,212,92
RQ 208,238,20480
v_mul_f32 v230, s71, v230
PW 72,188,224
RQ 212,239,20480
v_mul_f32 v229, s71, v229
PW 72,204,228
v_exp_f32 v229, v229
PW 76,192,224
v_exp_f32 v230, v230
PW 76,208,228
s_waitcnt lgkmcnt(3)
v_mul_f32 v228, s71, v228
v_mul_f32 v234, s71, v234
PW 92,192,216
v_exp_f32 v231, v231
PW 92,208,220
v_exp_f32 v234, v234
PW 88,188,216
v_mul_f32 v232, s71, v232
v_mul_f32 v233, s71, v233
PW 88,204,220
v_exp_f32 v228, v228
PW 84,184,216
s_waitcnt vmcnt(21) lgkmcnt(0)
v_exp_f32 v233, v233
PW 84,200,220
s_barrier
PW 80,180,216
L_s45_%=:
v_accvgpr_write_b32 a108, v224
v_accvgpr_write_b32 a99, v215
PW 80,196,220
RQ 112,244,0
CV 222,228,229
QC 248,192,0
RT 224,241,13312
QK 248,196,4
RT 228,241,13328
CV 223,230,231
QK 248,200,8
RT 230,241,19472
v_mul_f32 v235, s71, v235
QK 248,204,12
RT 226,241,19456
v_accvgpr_write_b32 a102, v218
QK 248,208,16
v_exp_f32 v235, v235
QK 248,212,20
RT 220,240,14352
QC 228,192,24
RT 218,240,20480
v_accvgpr_write_b32 a96, v212
QK 228,196,28
v_exp_f32 v232, v232
QK 228,200,32
RT 134,243,51216
CV 227,234,235
QK 228,204,36
v_mul_f32 v248, s71, v248
v_mul_f32 v250, s71, v250
QK 228,208,40
RQ 116,245,0
v_mul_f32 v249, s71, v249
QK 228,212,44
v_exp_f32 v249, v249
v_smfmac_f32_16x16x64_bf16 a[232:235], a[236:239], v[212:219], v236
s_waitcnt lgkmcnt(2)
RT 142,237,51216
CV 226,232,233
PW 96,196,224
RT 216,240,14336
v_mul_f32 v251, s71, v251
PW 100,200,224
v_exp_f32 v248, v248
v_mfma_f32_16x16x32_bf16 a[244:247], v[196:199], a[228:231], a[244:247]
v_mul_f32 v229, s71, v229
v_mul_f32 v231, s71, v231
v_mfma_f32_16x16x32_bf16 a[248:251], v[200:203], a[228:231], a[248:251]
v_exp_f32 v229, v229
QC 232,192,48
v_exp_f32 v251, v251
QK 232,196,52
v_exp_f32 v231, v231
QK 232,200,56
RT 128,243,45056
v_mul_f32 v230, s71, v230
QK 232,204,60
CV 180,248,249
v_mul_f32 v228, s71, v228
QK 232,208,64
v_exp_f32 v228, v228
QK 232,212,68
RT 222,240,20496
v_accvgpr_write_b32 a110, v226
v_smfmac_f32_16x16x64_bf16 a[232:235], a[240:243], v[220:227], v236
RQ 124,245,1024
PW 104,204,224
v_exp_f32 v250, v250
PW 108,208,224
RT 140,237,45072
v_mfma_f32_16x16x32_bf16 a[252:255], v[204:207], a[228:231], a[252:255]
RT 226,241,20480
CV 181,250,251
PW 112,208,228
v_exp_f32 v230, v230
QC 248,192,72
RQ 192,244,2048
v_mul_f32 v233, s71, v233
QK 248,196,76
RT 156,237,28688
v_mul_f32 v234, s71, v234
QK 248,200,80
v_exp_f32 v234, v234
QK 248,204,84
RQ 196,245,2048
v_mul_f32 v232, s71, v232
QK 248,208,88
RQ 120,244,1024
CV 185,230,231
QK 248,212,92
s_waitcnt lgkmcnt(4)
v_exp_f32 v232, v232
PW 128,208,216
RT 228,241,14352
CV 184,228,229
PW 124,204,216
RT 230,241,20496
v_mul_f32 v235, s71, v235
PW 144,208,220
v_exp_f32 v233, v233
PW 140,204,220
RT 150,243,34832
CV 188,232,233
QC 228,112,0
RT 152,237,28672
v_mul_f32 v251, s71, v251
QK 228,116,4
s_waitcnt lgkmcnt(4)
v_mul_f32 v249, s71, v249
QK 228,120,8
v_exp_f32 v249, v249
QK 228,124,12
RT 138,237,51200
v_mul_f32 v248, s71, v248
QK 228,192,16
v_exp_f32 v248, v248
QK 228,196,20
v_exp_f32 v251, v251
PW 120,200,216
RT 224,241,14336
v_mul_f32 v250, s71, v250
PW 116,196,216
v_exp_f32 v235, v235
PW 136,200,220
RQ 212,245,7168
CV 189,234,235
PW 132,196,220
RT 132,243,45072
v_mul_f32 v229, s71, v229
QC 232,112,24
CV 192,248,249
v_mul_f32 v230, s71, v230
QK 232,116,28
RT 154,237,34816
v_mul_f32 v231, s71, v231
QK 232,120,32
v_exp_f32 v250, v250
QK 232,124,36
v_exp_f32 v229, v229
)"
R"(
QK 232,192,40
RQ 200,244,6144
v_mul_f32 v228, s71, v228
QK 232,196,44
s_waitcnt lgkmcnt(2)
v_exp_f32 v230, v230
PW 148,196,224
v_exp_f32 v231, v231
PW 152,200,224
v_accvgpr_write_b32 a109, v225
CV 193,250,251
PW 164,196,228
v_exp_f32 v228, v228
PW 168,200,228
RT 130,243,51200
v_mul_f32 v233, s71, v233
QC 248,112,48
s_mov_b32 m0, s80
CV 183,230,231
v_mul_f32 v235, s71, v235
QK 248,116,52
RT 144,243,28672
v_accvgpr_write_b32 a111, v227
QK 248,120,56
v_exp_f32 v235, v235
QK 248,124,60
v_mul_f32 v232, s71, v232
v_mul_f32 v234, s71, v234
QK 248,192,64
RT 158,237,34832
CV 182,228,229
QK 248,196,68
v_exp_f32 v234, v234
PW 156,204,224
RQ 208,244,7168
CV 187,234,235
PW 160,208,224
v_exp_f32 v233, v233
PW 172,204,228
v_exp_f32 v232, v232
PW 176,208,228
RQ 204,245,6144
v_mul_f32 v251, s71, v251
QC 228,112,72
v_mul_f32 v248, s71, v248
v_mul_f32 v250, s71, v250
QK 228,116,76
RQ 112,244,8192
CV 186,232,233
QK 228,120,80
RQ 116,245,8192
v_mul_f32 v249, s71, v249
QK 228,124,84
v_exp_f32 v248, v248
QK 228,192,88
v_exp_f32 v251, v251
QK 228,196,92
v_exp_f32 v250, v250
v_smfmac_f32_16x16x64_bf16 a[232:235], a[236:239], v[180:187], v236
s_waitcnt lgkmcnt(2)
v_exp_f32 v249, v249
QC 232,200,0
RT 136,237,45056
CV 191,250,251
QK 232,204,4
RT 146,243,34816
CV 190,248,249
QK 232,208,8
v_mul_f32 v228, s71, v228
v_mul_f32 v229, s71, v229
QK 232,212,12
s_waitcnt lgkmcnt(2)
RQ 120,244,16384
v_mul_f32 v231, s71, v231
QK 232,112,16
v_exp_f32 v229, v229
QK 232,116,20
v_exp_f32 v231, v231
QC 248,200,24
RQ 124,245,16384
v_mul_f32 v230, s71, v230
QK 248,204,28
v_exp_f32 v230, v230
QK 248,208,32
v_exp_f32 v228, v228
QK 248,212,36
RQ 192,244,17408
CV 194,228,229
QK 248,112,40
CV 195,230,231
v_mul_f32 v233, s71, v233
v_mul_f32 v234, s71, v234
QK 248,116,44
v_mul_f32 v232, s71, v232
v_mul_f32 v235, s71, v235
v_smfmac_f32_16x16x64_bf16 a[232:235], a[240:243], v[188:195], v236
v_exp_f32 v234, v234
QC 228,200,48
v_exp_f32 v235, v235
QK 228,204,52
v_exp_f32 v233, v233
QK 228,208,56
v_exp_f32 v232, v232
QK 228,212,60
s_add_u32 s59, s70, 0
v_mul_f32 v249, s71, v249
v_mul_f32 v250, s71, v250
QK 228,112,64
CV 197,234,235
v_mul_f32 v251, s71, v251
QK 228,116,68
v_exp_f32 v251, v251
QK 0,96,128
s_waitcnt lgkmcnt(0)
RT 148,243,28688
v_mul_f32 v248, s71, v248
PW 0,180,144
CV 196,232,233
v_exp_f32 v250, v250
QC 232,200,72
v_exp_f32 v249, v249
QK 232,204,76
v_exp_f32 v248, v248
v_mul_f32 v228, s71, v228
QK 232,208,80
CV 201,250,251
v_mul_f32 v229, s71, v229
QK 232,212,84
RQ 196,245,17408
v_mul_f32 v231, s71, v231
QK 232,112,88
RQ 200,244,18432
v_mul_f32 v230, s71, v230
QK 232,116,92
RQ 204,245,18432
CV 200,248,249
QK 16,96,132
s_waitcnt lgkmcnt(3)
v_exp_f32 v230, v230
PW 16,180,148
v_exp_f32 v228, v228
QC 248,120,0
v_exp_f32 v229, v229
QK 248,124,4
v_exp_f32 v231, v231
QK 248,192,8
s_waitcnt lgkmcnt(1)
RQ 116,245,23552
CV 204,228,229
QK 248,196,12
CV 205,230,231
v_mul_f32 v234, s71, v234
QK 248,200,16
s_waitcnt lgkmcnt(1)
v_mul_f32 v232, s71, v232
v_mul_f32 v235, s71, v235
QK 248,204,20
RQ 212,245,22528
v_mul_f32 v233, s71, v233
QK 32,96,136
v_exp_f32 v234, v234
PW 32,180,152
v_exp_f32 v232, v232
QC 228,120,24
v_exp_f32 v235, v235
QK 228,124,28
v_exp_f32 v233, v233
CV 209,234,235
QK 228,192,32
v_mul_f32 v248, s71, v248
v_mul_f32 v251, s71, v251
QK 228,196,36
RQ 112,244,23552
v_mul_f32 v250, s71, v250
QK 228,200,40
v_exp_f32 v251, v251
QK 228,204,44
v_exp_f32 v248, v248
QK 48,96,140
v_exp_f32 v250, v250
PW 48,180,156
RQ 208,244,22528
CV 208,232,233
QC 232,120,48
v_mul_f32 v249, s71, v249
CV 199,250,251
QK 232,124,52
v_mul_f32 v228, s71, v228
v_mul_f32 v230, s71, v230
v_mul_f32 v231, s71, v231
QK 232,192,56
v_exp_f32 v249, v249
QK 232,196,60
CV 198,248,249
v_mul_f32 v229, s71, v229
QK 232,200,64
v_exp_f32 v230, v230
QK 232,204,68
v_exp_f32 v228, v228
QC 248,120,72
v_exp_f32 v231, v231
QK 248,124,76
RQ 120,244,24576
CV 203,230,231
QK 248,192,80
v_exp_f32 v229, v229
QK 248,196,84
v_mul_f32 v233, s71, v233
v_mul_f32 v234, s71, v234
QK 248,200,88
RQ 124,245,24576
CV 202,228,229
QK 248,204,92
s_waitcnt vmcnt(4) lgkmcnt(2)
v_mul_f32 v232, s71, v232
v_mul_f32 v235, s71, v235
v_smfmac_f32_16x16x64_bf16 a[232:235], a[236:239], v[196:203], v236
s_barrier
QC 228,208,0
v_exp_f32 v232, v232
QK 228,212,4
v_exp_f32 v233, v233
QK 228,112,8
RT 168,237,0
v_mul_f32 v250, s71, v250
QK 228,116,12
s_waitcnt lgkmcnt(1)
RT 160,243,0
v_exp_f32 v235, v235
QK 228,120,16
RT 166,243,6160
v_exp_f32 v234, v234
CV 206,232,233
QK 228,124,20
RQ 200,238,25600
CV 207,234,235
v_mul_f32 v248, s71, v248
QC 232,208,24
v_mul_f32 v249, s71, v249
v_exp_f32 v250, v250
QK 232,212,28
v_mul_f32 v251, s71, v251
v_exp_f32 v248, v248
v_exp_f32 v249, v249
QK 232,112,32
v_exp_f32 v251, v251
QK 232,116,36
CV 210,248,249
v_mul_f32 v229, s71, v229
QK 232,120,40
RT 162,243,6144
CV 211,250,251
v_mul_f32 v230, s71, v230
v_exp_f32 v230, v230
QK 232,124,44
RT 164,243,16
v_mul_f32 v228, s71, v228
v_smfmac_f32_16x16x64_bf16 a[232:235], a[240:243], v[204:211], v236
RQ 204,239,25600
v_mul_f32 v231, s71, v231
QC 248,208,48
RT 182,243,18448
v_exp_f32 v229, v229
QK 248,212,52
v_exp_f32 v231, v231
QK 248,112,56
RQ 196,239,24576
v_mul_f32 v233, s71, v233
QK 248,116,60
RT 174,237,6160
v_exp_f32 v228, v228
CV 213,230,231
QK 248,120,64
RQ 192,238,24576
CV 212,228,229
QK 248,124,68
s_waitcnt lgkmcnt(3)
RT 180,243,12304
v_mul_f32 v234, s71, v234
PW 0,196,160
v_exp_f32 v233, v233
QC 228,208,72
RQ 208,238,26624
v_exp_f32 v234, v234
QK 228,212,76
RT 172,237,16
v_mul_f32 v232, s71, v232
QK 228,112,80
v_exp_f32 v232, v232
QK 228,116,84
RT 170,237,6144
v_mul_f32 v235, s71, v235
QK 228,120,88
RQ 212,239,26624
v_mul_f32 v251, s71, v251
QK 228,124,92
v_exp_f32 v235, v235
CV 216,232,233
v_exp_f32 v251, v251
PW 16,196,164
s_waitcnt lgkmcnt(2)
CV 217,234,235
v_mul_f32 v249, s71, v249
QC 232,192,0
RT 220,243,29696
v_mul_f32 v250, s71, v250
v_exp_f32 v250, v250
QK 232,196,4
RT 176,243,12288
v_mul_f32 v248, s71, v248
CV 221,250,251
QK 232,200,8
v_exp_f32 v248, v248
v_mul_f32 v229, s71, v229
QK 232,204,12
RT 184,237,12288
v_mul_f32 v228, s71, v228
v_exp_f32 v228, v228
QK 232,208,16
s_waitcnt lgkmcnt(2)
RT 178,243,18432
v_exp_f32 v249, v249
QK 232,212,20
RT 188,237,12304
CV 220,248,249
PW 32,196,168
v_mul_f32 v230, s71, v230
v_mul_f32 v231, s71, v231
v_exp_f32 v231, v231
QC 248,192,24
v_exp_f32 v229, v229
QK 248,196,28
v_exp_f32 v230, v230
QK 248,200,32
RT 122,243,7184
v_mul_f32 v234, s71, v234
QK 248,204,36
RT 190,237,18448
CV 225,230,231
QK 248,208,40
v_mul_f32 v233, s71, v233
v_mul_f32 v235, s71, v235
QK 248,212,44
RT 186,237,18432
CV 224,228,229
PW 48,196,172
v_exp_f32 v235, v235
QC 228,192,48
RT 222,243,35840
v_mul_f32 v232, s71, v232
QK 228,196,52
RT 226,243,7168
v_exp_f32 v233, v233
QK 228,200,56
v_exp_f32 v232, v232
QK 228,204,60
v_exp_f32 v234, v234
v_mul_f32 v248, s71, v248
v_mul_f32 v250, s71, v250
QK 228,208,64
CV 214,232,233
CV 215,234,235
QK 228,212,68
s_waitcnt lgkmcnt(1)
v_exp_f32 v248, v248
PW 0,212,176
v_mul_f32 v249, s71, v249
v_mul_f32 v251, s71, v251
PW 16,212,180
v_exp_f32 v250, v250
QC 232,192,72
v_exp_f32 v249, v249
QK 232,196,76
v_exp_f32 v251, v251
QK 232,200,80
RT 218,243,52224
CV 218,248,249
CV 219,250,251
QK 232,204,84
RT 200,237,1024
v_mul_f32 v230, s71, v230
v_smfmac_f32_16x16x64_bf16 a[232:235], a[236:239], v[212:219], v236
RT 124,243,13328
v_mul_f32 v229, s71, v229
QK 232,208,88
v_exp_f32 v230, v230
QK 232,212,92
RT 230,243,19456
v_accvgpr_write_b32 a107, v223
PW 32,212,184
v_exp_f32 v229, v229
PW 48,212,188
L_c21_%=:
s_cbranch_execz L_ret2_%=
v_accvgpr_read_b32 v252, a232
v_accvgpr_read_b32 v248, a233
v_add_f32 v252, v252, v248
v_rcp_f32 v252, v252
QK 4,100,128
ds_write_addtid_b32 v252 offset:65280
)"
R"(
v_mul_f32 v228, s71, v228
PW 4,184,144
ds_read_b128 v[248:251], v254 offset:0
v_accvgpr_write_b32 a106, v222
PW 4,200,160
RT 116,243,29712
v_mul_f32 v232, s71, v232
PW 4,216,176
RT 228,243,13312
v_mul_f32 v234, s71, v234
PW 20,216,180
RT 114,243,52240
v_mul_f32 v235, s71, v235
PW 20,200,164
s_mov_b32 m0, s80
v_exp_f32 v235, v235
PW 20,184,148
RT 210,237,52240
v_mul_f32 v231, s71, v231
v_mul_f32 v233, s71, v233
QK 20,100,132
s_waitcnt lgkmcnt(4)
RT 212,237,29712
v_exp_f32 v232, v232
v_exp_f32 v234, v234
v_pk_mul_f32 v[0:1], v[0:1], v[248:249]
v_pk_mul_f32 v[2:3], v[2:3], v[250:251]
v_pk_mul_f32 v[16:17], v[16:17], v[248:249]
v_pk_mul_f32 v[18:19], v[18:19], v[250:251]
CV 0,0,16
QK 36,100,136
RT 120,243,1040
CV 16,2,18
PW 36,184,152
RT 206,237,19456
v_exp_f32 v228, v228
CV 2,1,17
CV 18,3,19
v_pk_mul_f32 v[32:33], v[32:33], v[248:249]
v_pk_mul_f32 v[34:35], v[34:35], v[250:251]
v_pk_mul_f32 v[48:49], v[48:49], v[248:249]
v_pk_mul_f32 v[50:51], v[50:51], v[250:251]
CV 3,33,49
PW 36,200,168
ds_read_b128 v[248:251], v254 offset:64
CV 1,32,48
PW 36,216,184
RT 198,237,35840
ds_write_b128 v246, v[0:3] offset:0
v_exp_f32 v233, v233
PW 52,216,188
v_exp_f32 v231, v231
PW 52,200,172
RT 0,247,0
CV 226,232,233
PW 52,184,156
RT 2,247,64
CV 222,228,229
QK 52,100,140
CV 17,34,50
CV 227,234,235
QK 8,104,128
RT 126,243,19472
CV 19,35,51
PW 8,188,144
s_waitcnt lgkmcnt(3)
RT 192,237,46080
ds_write_b128 v246, v[16:19] offset:1280
v_pk_mul_f32 v[4:5], v[4:5], v[248:249]
v_pk_mul_f32 v[6:7], v[6:7], v[250:251]
v_pk_mul_f32 v[20:21], v[20:21], v[248:249]
v_pk_mul_f32 v[22:23], v[22:23], v[250:251]
v_pk_mul_f32 v[36:37], v[36:37], v[248:249]
v_pk_mul_f32 v[38:39], v[38:39], v[250:251]
CV 223,230,231
PW 8,204,160
CV 4,4,20
CV 20,6,22
v_smfmac_f32_16x16x64_bf16 a[232:235], a[240:243], v[220:227], v236
s_waitcnt lgkmcnt(2)
RT 92,237,13328
CV 22,7,23
PW 8,220,176
buffer_store_dwordx4 a[0:3], v242, s[52:55], s59 offen offset:0 nt
RT 6,247,1344
CV 6,5,21
v_pk_mul_f32 v[52:53], v[52:53], v[248:249]
v_pk_mul_f32 v[54:55], v[54:55], v[250:251]
CV 5,36,52
PW 24,220,180
RT 4,247,1280
CV 7,37,53
PW 24,204,164
ds_write_b128 v246, v[4:7] offset:0
PW 24,188,148
v_accvgpr_read_b32 v252, a232
v_accvgpr_read_b32 v228, a233
QK 24,104,132
v_add_f32 v252, v252, v228
v_rcp_f32 v252, v252
QK 40,104,136
ds_write_addtid_b32 v252 offset:65280
PW 40,188,152
ds_read_b128 v[248:251], v254 offset:128
PW 40,204,168
s_waitcnt lgkmcnt(3)
RT 216,243,46080
CV 21,38,54
PW 40,220,184
buffer_store_dwordx4 a[4:7], v242, s[52:55], s59 offen offset:768 nt
RT 8,247,0
RT 10,247,64
PW 56,220,188
s_add_u32 s59, s70, 6144
RT 214,237,35856
CV 23,39,55
PW 56,204,172
RT 224,243,1024
PW 56,188,156
RT 204,237,13312
QK 56,104,140
s_waitcnt lgkmcnt(5)
ds_write_b128 v246, v[20:23] offset:1280
v_pk_mul_f32 v[8:9], v[8:9], v[248:249]
v_pk_mul_f32 v[10:11], v[10:11], v[250:251]
v_pk_mul_f32 v[24:25], v[24:25], v[248:249]
v_pk_mul_f32 v[26:27], v[26:27], v[250:251]
CV 8,8,24
QK 12,108,128
RT 12,247,1280
PW 12,192,144
RT 112,243,46096
CV 24,10,26
PW 12,208,160
RT 14,247,1344
PW 12,224,176
s_waitcnt lgkmcnt(4)
buffer_store_dwordx4 a[8:11], v242, s[52:55], s59 offen offset:0 nt
RT 88,237,1040
v_pk_mul_f32 v[40:41], v[40:41], v[248:249]
v_pk_mul_f32 v[42:43], v[42:43], v[250:251]
v_pk_mul_f32 v[56:57], v[56:57], v[248:249]
v_pk_mul_f32 v[58:59], v[58:59], v[250:251]
CV 26,11,27
PW 28,224,180
ds_read_b128 v[248:251], v254 offset:192
PW 28,208,164
RT 208,237,46096
PW 28,192,148
RT 194,237,52224
CV 10,9,25
QK 28,108,132
RT 118,243,35856
QK 44,108,136
RT 202,237,7168
PW 44,192,152
RT 166,243,8208
CV 11,41,57
PW 44,208,168
RT 178,243,20480
CV 9,40,56
PW 44,224,184
s_waitcnt lgkmcnt(5)
buffer_store_dwordx4 a[12:15], v242, s[52:55], s59 offen offset:768 nt
ds_write_b128 v246, v[8:11] offset:0
v_pk_mul_f32 v[12:13], v[12:13], v[248:249]
v_pk_mul_f32 v[14:15], v[14:15], v[250:251]
v_pk_mul_f32 v[28:29], v[28:29], v[248:249]
v_pk_mul_f32 v[30:31], v[30:31], v[250:251]
CV 12,12,28
PW 60,224,188
s_add_u32 s59, s70, 12288
RT 16,247,0
PW 60,208,172
RT 18,247,64
CV 27,43,59
PW 60,192,156
RT 90,237,7184
QK 60,108,140
RT 136,237,47104
CV 28,14,30
QK 64,96,216
RT 94,237,19472
CV 25,42,58
PW 64,180,220
RT 196,237,29696
v_cmp_gt_u32 s[78:79], s74, v252
PW 64,196,224
RT 146,243,36864
CV 14,13,29
PW 64,212,228
s_waitcnt lgkmcnt(5)
buffer_store_dwordx4 a[16:19], v242, s[52:55], s59 offen offset:0 nt
ds_write_b128 v246, v[24:27] offset:1280
v_pk_mul_f32 v[44:45], v[44:45], v[248:249]
v_pk_mul_f32 v[46:47], v[46:47], v[250:251]
v_pk_mul_f32 v[60:61], v[60:61], v[248:249]
v_pk_mul_f32 v[62:63], v[62:63], v[250:251]
CV 30,15,31
PW 80,212,124
ds_read_b128 v[248:251], v254 offset:0
PW 80,196,120
RT 20,247,1280
CV 13,44,60
PW 80,180,116
RT 22,247,1344
CV 15,45,61
QK 80,96,112
RT 172,237,2064
CV 31,47,63
QK 96,96,192
s_waitcnt lgkmcnt(4)
RT 128,243,47104
PW 96,180,196
ds_write_b128 v246, v[12:15] offset:0
PW 96,196,200
CV 29,46,62
PW 96,212,204
s_waitcnt lgkmcnt(3)
buffer_store_dwordx4 a[20:23], v242, s[52:55], s59 offen offset:768 nt
RT 26,247,64
v_pk_mul_f32 v[64:65], v[64:65], v[248:249]
v_pk_mul_f32 v[66:67], v[66:67], v[250:251]
v_pk_mul_f32 v[80:81], v[80:81], v[248:249]
v_pk_mul_f32 v[82:83], v[82:83], v[250:251]
CV 64,64,80
v_mfma_f32_16x16x32_bf16 a[244:247], v[212:215], a[92:95], a[244:247]
RT 24,247,0
v_mfma_f32_16x16x32_bf16 a[244:247], v[196:199], a[88:91], a[244:247]
RT 190,237,20496
CV 80,66,82
v_mfma_f32_16x16x32_bf16 a[244:247], v[180:183], a[212:215], a[244:247]
RT 182,243,20496
v_mfma_f32_16x16x32_bf16 a[244:247], a[96:99], a[208:211], a[244:247]
RT 180,243,14352
CV 82,67,83
QK 68,100,216
RT 156,237,30736
PW 68,184,220
s_add_u32 s59, s70, 18432
RT 162,243,8192
CV 66,65,81
PW 68,200,224
s_waitcnt lgkmcnt(5)
ds_write_b128 v246, v[28:31] offset:1280
PW 68,216,228
buffer_store_dwordx4 a[24:27], v242, s[52:55], s59 offen offset:0 nt
v_accvgpr_read_b32 v65, a244
v_accvgpr_read_b32 v83, a247
v_mul_f32 v65, v65, v248
v_pk_mul_f32 v[96:97], v[96:97], v[248:249]
v_pk_mul_f32 v[98:99], v[98:99], v[250:251]
v_mul_f32 v83, v83, v251
PW 84,216,124
RT 30,247,1344
v_accvgpr_read_b32 v81, a246
PW 84,200,120
RT 28,247,1280
v_mul_f32 v81, v81, v250
PW 84,184,116
v_accvgpr_read_b32 v67, a245
v_mul_f32 v67, v67, v249
QK 84,100,112
ds_read_b128 v[248:251], v254 offset:64
CV 65,96,65
QK 100,100,192
RT 130,243,53248
CV 67,97,67
PW 100,184,196
ds_write_b128 v246, v[64:67] offset:0
PW 100,200,200
CV 83,99,83
PW 100,216,204
s_waitcnt lgkmcnt(3)
buffer_store_dwordx4 a[28:31], v242, s[52:55], s59 offen offset:768 nt
RT 32,247,0
RT 34,247,64
v_mfma_f32_16x16x32_bf16 a[248:251], v[216:219], a[92:95], a[248:251]
RT 138,237,53248
CV 81,98,81
v_mfma_f32_16x16x32_bf16 a[248:251], v[200:203], a[88:91], a[248:251]
RT 148,243,30736
v_mfma_f32_16x16x32_bf16 a[248:251], v[184:187], a[212:215], a[248:251]
s_waitcnt lgkmcnt(4)
ds_write_b128 v246, v[80:83] offset:1280
v_pk_mul_f32 v[68:69], v[68:69], v[248:249]
v_pk_mul_f32 v[70:71], v[70:71], v[250:251]
v_pk_mul_f32 v[84:85], v[84:85], v[248:249]
v_pk_mul_f32 v[86:87], v[86:87], v[250:251]
CV 68,68,84
v_mfma_f32_16x16x32_bf16 a[248:251], a[100:103], a[208:211], a[248:251]
RT 36,247,1280
QK 72,104,216
RT 154,237,36864
CV 84,70,86
PW 72,188,220
s_add_u32 s59, s70, 0
RT 188,237,14352
CV 86,71,87
PW 72,204,224
s_waitcnt lgkmcnt(4)
RT 38,247,1344
CV 70,69,85
PW 72,220,228
buffer_store_dwordx4 a[32:35], v242, s[52:55], s59 offen offset:128 nt
RT 168,237,2048
v_accvgpr_read_b32 v71, a249
v_pk_mul_f32 v[100:101], v[100:101], v[248:249]
v_pk_mul_f32 v[102:103], v[102:103], v[250:251]
v_mul_f32 v71, v71, v249
PW 88,220,124
RT 184,237,14336
v_accvgpr_read_b32 v69, a248
PW 88,204,120
v_accvgpr_read_b32 v85, a250
v_accvgpr_read_b32 v87, a251
PW 88,188,116
v_mul_f32 v69, v69, v248
v_mul_f32 v87, v87, v251
QK 88,104,112
RT 144,243,30720
v_mul_f32 v85, v85, v250
QK 104,104,192
ds_read_b128 v[248:251], v254 offset:128
CV 69,100,69
PW 104,188,196
RT 176,243,14336
CV 71,101,71
PW 104,204,200
RT 134,243,53264
PW 104,220,204
s_waitcnt lgkmcnt(4)
buffer_store_dwordx4 a[36:39], v242, s[52:55], s59 offen offset:896 nt
ds_write_b128 v246, v[68:71] offset:0
CV 87,103,87
v_mfma_f32_16x16x32_bf16 a[252:255], v[220:223], a[92:95], a[252:255]
CV 85,102,85
v_mfma_f32_16x16x32_bf16 a[252:255], v[204:207], a[88:91], a[252:255]
RT 40,247,0
v_mfma_f32_16x16x32_bf16 a[252:255], v[188:191], a[212:215], a[252:255]
s_waitcnt lgkmcnt(2)
RT 42,247,64
v_mfma_f32_16x16x32_bf16 a[252:255], a[104:107], a[208:211], a[252:255]
s_add_u32 s59, s70, 6144
ds_write_b128 v246, v[84:87] offset:1280
v_pk_mul_f32 v[72:73], v[72:73], v[248:249]
v_pk_mul_f32 v[74:75], v[74:75], v[250:251]
v_pk_mul_f32 v[88:89], v[88:89], v[248:249]
v_pk_mul_f32 v[90:91], v[90:91], v[250:251]
CV 72,72,88
QK 76,108,216
RT 44,247,1280
PW 76,192,220
RT 46,247,1344
CV 88,74,90
PW 76,208,224
s_waitcnt lgkmcnt(3)
RT 132,243,47120
CV 74,73,89
PW 76,224,228
buffer_store_dwordx4 a[40:43], v242, s[52:55], s59 offen offset:128 nt
RT 170,237,8192
CV 90,75,91
v_pk_mul_f32 v[104:105], v[104:105], v[248:249]
v_pk_mul_f32 v[106:107], v[106:107], v[250:251]
v_accvgpr_read_b32 v89, a254
PW 92,224,124
v_accvgpr_read_b32 v73, a252
v_accvgpr_read_b32 v91, a255
PW 92,208,120
v_mul_f32 v73, v73, v248
v_mul_f32 v91, v91, v251
PW 92,192,116
RT 160,243,2048
v_mul_f32 v89, v89, v250
QK 92,108,112
RT 150,243,36880
v_accvgpr_read_b32 v75, a253
QK 108,108,192
RT 174,237,8208
v_mul_f32 v75, v75, v249
PW 108,192,196
ds_read_b128 v[248:251], v254 offset:192
CV 73,104,73
PW 108,208,200
RT 164,243,2064
CV 75,105,75
PW 108,224,204
s_waitcnt lgkmcnt(5)
buffer_store_dwordx4 a[44:47], v242, s[52:55], s59 offen offset:896 nt
ds_write_b128 v246, v[72:75] offset:0
PW 112,224,92
s_add_u32 s59, s70, 12288
PW 112,208,88
RT 48,247,0
PW 112,192,212
RT 50,247,64
CV 91,107,91
QK 112,108,208
RT 152,237,30720
)"
R"(
CV 89,106,89
QK 116,96,128
s_waitcnt lgkmcnt(4)
ds_write_b128 v246, v[88:91] offset:1280
v_pk_mul_f32 v[76:77], v[76:77], v[248:249]
v_pk_mul_f32 v[78:79], v[78:79], v[250:251]
v_pk_mul_f32 v[92:93], v[92:93], v[248:249]
v_pk_mul_f32 v[94:95], v[94:95], v[250:251]
v_cmp_lt_u32 vcc, s75, v252
PW 116,180,144
RT 186,237,20480
PW 116,196,160
RT 52,247,1280
PW 116,212,176
s_waitcnt lgkmcnt(3)
buffer_store_dwordx4 a[48:51], v242, s[52:55], s59 offen offset:128 nt
RT 54,247,1344
v_pk_mul_f32 v[108:109], v[108:109], v[248:249]
v_pk_mul_f32 v[110:111], v[110:111], v[250:251]
v_pk_mul_f32 v[112:113], v[112:113], v[248:249]
v_pk_mul_f32 v[114:115], v[114:115], v[250:251]
CV 76,76,92
PW 132,212,180
ds_read_b128 v[248:251], v254 offset:0
PW 132,196,164
RT 140,237,47120
PW 132,180,148
RT 158,237,36880
CV 92,78,94
QK 132,96,132
RT 142,237,53264
CV 78,77,93
QK 148,96,136
CV 94,79,95
CV 77,108,112
PW 148,180,152
CV 95,111,115
PW 148,196,168
s_waitcnt lgkmcnt(3)
CV 79,109,113
PW 148,212,184
buffer_store_dwordx4 a[52:55], v242, s[52:55], s59 offen offset:896 nt
ds_write_b128 v246, v[76:79] offset:0
v_pk_mul_f32 v[116:117], v[116:117], v[248:249]
v_pk_mul_f32 v[118:119], v[118:119], v[250:251]
v_pk_mul_f32 v[132:133], v[132:133], v[248:249]
v_pk_mul_f32 v[134:135], v[134:135], v[250:251]
CV 93,110,114
PW 164,212,188
RT 58,247,64
PW 164,196,172
s_waitcnt lgkmcnt(2)
RT 56,247,0
CV 116,116,132
PW 164,180,156
ds_write_b128 v246, v[92:95] offset:1280
QK 164,96,140
CV 132,118,134
CV 118,117,133
QK 120,100,128
s_add_u32 s59, s70, 18432
PW 120,184,144
RT 62,247,1344
CV 134,119,135
PW 120,200,160
PW 120,216,176
s_waitcnt lgkmcnt(2)
buffer_store_dwordx4 a[56:59], v242, s[52:55], s59 offen offset:128 nt
RT 60,247,1280
v_pk_mul_f32 v[148:149], v[148:149], v[248:249]
v_pk_mul_f32 v[150:151], v[150:151], v[250:251]
v_pk_mul_f32 v[164:165], v[164:165], v[248:249]
v_pk_mul_f32 v[166:167], v[166:167], v[250:251]
CV 117,148,164
PW 136,216,180
PW 136,200,164
ds_read_b128 v[248:251], v254 offset:64
PW 136,184,148
CV 119,149,165
QK 136,100,132
ds_write_b128 v246, v[116:119] offset:0
QK 152,100,136
CV 133,150,166
PW 152,184,152
PW 152,200,168
s_waitcnt lgkmcnt(1)
PW 152,216,184
buffer_store_dwordx4 a[60:63], v242, s[52:55], s59 offen offset:896 nt
RT 66,247,64
CV 135,151,167
PW 168,216,188
RT 64,247,0
PW 168,200,172
s_add_u32 s59, s70, 0
ds_write_b128 v246, v[132:135] offset:1280
v_pk_mul_f32 v[120:121], v[120:121], v[248:249]
v_pk_mul_f32 v[122:123], v[122:123], v[250:251]
v_pk_mul_f32 v[136:137], v[136:137], v[248:249]
v_pk_mul_f32 v[138:139], v[138:139], v[250:251]
CV 120,120,136
PW 168,184,156
CV 136,122,138
QK 168,100,140
QK 124,104,128
PW 124,188,144
PW 124,204,160
RT 68,247,1280
CV 138,123,139
PW 124,220,176
s_waitcnt lgkmcnt(1)
buffer_store_dwordx4 a[64:67], v242, s[52:55], s59 offen offset:256 nt
RT 70,247,1344
v_pk_mul_f32 v[152:153], v[152:153], v[248:249]
v_pk_mul_f32 v[154:155], v[154:155], v[250:251]
v_pk_mul_f32 v[168:169], v[168:169], v[248:249]
v_pk_mul_f32 v[170:171], v[170:171], v[250:251]
CV 139,155,171
PW 140,220,180
PW 140,204,164
CV 122,121,137
CV 121,152,168
PW 140,188,148
s_or_b64 s[78:79], s[78:79], vcc
s_cselect_b32 s73, 4, 0
CV 123,153,169
QK 140,104,132
CV 137,154,170
QK 156,104,136
s_or_b32 s72, s72, s73
ds_read_b128 v[248:251], v254 offset:128
PW 156,188,152
ds_write_b128 v246, v[120:123] offset:0
PW 156,204,168
s_waitcnt lgkmcnt(2)
PW 156,220,184
buffer_store_dwordx4 a[68:71], v242, s[52:55], s59 offen offset:1024 nt
L_c22_%=:
s_cbranch_execz L_ret2_%=
RT 74,247,64
PW 172,220,188
s_add_u32 s59, s70, 6144
RT 72,247,0
PW 172,204,172
PW 172,188,156
QK 172,104,140
s_waitcnt lgkmcnt(2)
ds_write_b128 v246, v[136:139] offset:1280
v_pk_mul_f32 v[124:125], v[124:125], v[248:249]
v_pk_mul_f32 v[126:127], v[126:127], v[250:251]
v_pk_mul_f32 v[140:141], v[140:141], v[248:249]
v_pk_mul_f32 v[142:143], v[142:143], v[250:251]
CV 124,124,140
QK 128,108,128
CV 140,126,142
PW 128,192,144
RT 76,247,1280
PW 128,208,160
RT 78,247,1344
CV 142,127,143
PW 128,224,176
s_waitcnt lgkmcnt(3)
buffer_store_dwordx4 a[72:75], v242, s[52:55], s59 offen offset:256 nt
CV 126,125,141
v_pk_mul_f32 v[156:157], v[156:157], v[248:249]
v_pk_mul_f32 v[158:159], v[158:159], v[250:251]
v_pk_mul_f32 v[172:173], v[172:173], v[248:249]
v_pk_mul_f32 v[174:175], v[174:175], v[250:251]
CV 127,157,173
PW 144,224,180
ds_read_b128 v[248:251], v254 offset:192
CV 125,156,172
PW 144,208,164
ds_write_b128 v246, v[124:127] offset:0
PW 144,192,148
CV 141,158,174
CV 143,159,175
QK 144,108,132
RT 80,247,0
ds_write_b128 v246, v[140:143] offset:1280
QK 160,108,136
PW 160,192,152
RT 86,247,1344
PW 160,208,168
RT 82,247,64
RT 84,247,1280
PW 160,224,184
s_waitcnt lgkmcnt(4)
buffer_store_dwordx4 a[76:79], v242, s[52:55], s59 offen offset:1024 nt
v_pk_mul_f32 v[128:129], v[128:129], v[248:249]
v_pk_mul_f32 v[130:131], v[130:131], v[250:251]
v_pk_mul_f32 v[144:145], v[144:145], v[248:249]
v_pk_mul_f32 v[146:147], v[146:147], v[250:251]
CV 128,128,144
PW 176,224,188
s_add_u32 s59, s70, 12288
CV 144,130,146
PW 176,208,172
CV 130,129,145
PW 176,192,156
CV 146,131,147
QK 176,108,140
s_waitcnt lgkmcnt(0)
buffer_store_dwordx4 a[80:83], v242, s[52:55], s59 offen offset:256 nt
buffer_store_dwordx4 a[84:87], v242, s[52:55], s59 offen offset:1024 nt
v_pk_mul_f32 v[160:161], v[160:161], v[248:249]
v_pk_mul_f32 v[162:163], v[162:163], v[250:251]
s_nop 3
v_pk_mul_f32 v[176:177], v[176:177], v[248:249]
v_pk_mul_f32 v[178:179], v[178:179], v[250:251]
CV 129,160,176
CV 131,161,177
ds_write_b128 v246, v[128:131] offset:0
CV 145,162,178
CV 147,163,179
ds_write_b128 v246, v[144:147] offset:1280
RT 88,247,0
RT 90,247,64
RT 92,247,1280
RT 94,247,1344
s_add_u32 s59, s70, 18432
s_waitcnt lgkmcnt(2)
buffer_store_dwordx4 a[88:91], v242, s[52:55], s59 offen offset:256 nt
s_waitcnt lgkmcnt(0)
buffer_store_dwordx4 a[92:95], v242, s[52:55], s59 offen offset:1024 nt
s_cmp_lg_u32 s72, 0
s_cbranch_scc1 L_fb_%=
s_endpgm
L_fb_%=:
s_waitcnt vmcnt(0)
s_mov_b32 %0, s72
L_end_%=:
.purgem PV
.purgem PZ
.purgem PW
.purgem PX
.purgem QK
.purgem QZ
.purgem QC
.purgem RQ
.purgem RT
.purgem CV
)"

    : "=s"(flags)
    : "s"(__builtin_amdgcn_kernarg_segment_ptr()), "s"(bid), "s"(w), "s"((unsigned)(size_t)lds)
    : "v0","v1","v2","v3","v4","v5","v6","v7","v8","v9","v10","v11","v12","v13","v14","v15","v16","v17","v18","v19","v20","v21","v22","v23","v24","v25","v26","v27","v28","v29","v30","v31","v32","v33","v34","v35","v36","v37","v38","v39","v40","v41","v42","v43","v44","v45","v46","v47","v48","v49","v50","v51","v52","v53","v54","v55","v56","v57","v58","v59","v60","v61","v62","v63","v64","v65","v66","v67","v68","v69","v70","v71","v72","v73","v74","v75","v76","v77","v78","v79","v80","v81","v82","v83","v84","v85","v86","v87","v88","v89","v90","v91","v92","v93","v94","v95","v96","v97","v98","v99","v100","v101","v102","v103","v104","v105","v106","v107","v108","v109","v110","v111","v112","v113","v114","v115","v116","v117","v118","v119","v120","v121","v122","v123","v124","v125","v126","v127","v128","v129","v130","v131","v132","v133","v134","v135","v136","v137","v138","v139","v140","v141","v142","v143","v144","v145","v146","v147","v148","v149","v150","v151","v152","v153","v154","v155","v156","v157","v158","v159","v160","v161","v162","v163","v164","v165","v166","v167","v168","v169","v170","v171","v172","v173","v174","v175","v176","v177","v178","v179","v180","v181","v182","v183","v184","v185","v186","v187","v188","v189","v190","v191","v192","v193","v194","v195","v196","v197","v198","v199","v200","v201","v202","v203","v204","v205","v206","v207","v208","v209","v210","v211","v212","v213","v214","v215","v216","v217","v218","v219","v220","v221","v222","v223","v224","v225","v226","v227","v228","v229","v230","v231","v232","v233","v234","v235","v236","v237","v238","v239","v240","v241","v242","v243","v244","v245","v246","v247","v248","v249","v250","v251","v252","v253","v254","v255","a0","a1","a2","a3","a4","a5","a6","a7","a8","a9","a10","a11","a12","a13","a14","a15","a16","a17","a18","a19","a20","a21","a22","a23","a24","a25","a26","a27","a28","a29","a30","a31","a32","a33","a34","a35","a36","a37","a38","a39","a40","a41","a42","a43","a44","a45","a46","a47","a48","a49","a50","a51","a52","a53","a54","a55","a56","a57","a58","a59","a60","a61","a62","a63","a64","a65","a66","a67","a68","a69","a70","a71","a72","a73","a74","a75","a76","a77","a78","a79","a80","a81","a82","a83","a84","a85","a86","a87","a88","a89","a90","a91","a92","a93","a94","a95","a96","a97","a98","a99","a100","a101","a102","a103","a104","a105","a106","a107","a108","a109","a110","a111","a112","a113","a114","a115","a116","a117","a118","a119","a120","a121","a122","a123","a124","a125","a126","a127","a128","a129","a130","a131","a132","a133","a134","a135","a136","a137","a138","a139","a140","a141","a142","a143","a144","a145","a146","a147","a148","a149","a150","a151","a152","a153","a154","a155","a156","a157","a158","a159","a160","a161","a162","a163","a164","a165","a166","a167","a168","a169","a170","a171","a172","a173","a174","a175","a176","a177","a178","a179","a180","a181","a182","a183","a184","a185","a186","a187","a188","a189","a190","a191","a192","a193","a194","a195","a196","a197","a198","a199","a200","a201","a202","a203","a204","a205","a206","a207","a208","a209","a210","a211","a212","a213","a214","a215","a216","a217","a218","a219","a220","a221","a222","a223","a224","a225","a226","a227","a228","a229","a230","a231","a232","a233","a234","a235","a236","a237","a238","a239","a240","a241","a242","a243","a244","a245","a246","a247","a248","a249","a250","a251","a252","a253","a254","a255","s40","s41","s42","s43","s44","s45","s46","s47","s48","s49","s50","s51","s52","s53","s54","s55","s56","s57","s58","s59","s60","s61","s62","s63","s64","s65","s66","s67","s68","s69","s70","s71","s72","s73","s74","s75","s76","s77","s78","s79","s80","s81","s82","s83","s84","s85","s86","s87","s88","s89","s90","s91","s92","s93","s94","s95","s96","s97","vcc","scc", "memory");
  if (flags) {
    const unsigned xcd = bid & 7, j = bid >> 3;
    const unsigned t0 = j < 30 ? j : 60 + j, t1 = j < 30 ? j + 30 : 63 + j, t2 = j < 30 ? j + 60 : 3 * j + 2;
    int lane;
    asm volatile("v_mbcnt_lo_u32_b32 %0, -1, 0\n v_mbcnt_hi_u32_b32 %0, -1, %0\n s_nop 1" : "=v"(lane));
    const float scale = 0.07216878364870322f;
    for (int b = 0; b < 3; ++b) {
      if (!((flags >> b) & 1)) continue;
      const unsigned t = b == 0 ? t0 : b == 1 ? t1 : t2, h = t / 3;
      const unsigned qo = (h * 8 + xcd) * 294912u + (t - 3 * h) * 98304u + w * 24576u, ko = (h * 8 + xcd) * 196608u;
      const unsigned* kp = (const unsigned*)(k + ko);
      const unsigned short* vp = (const unsigned short*)(v + ko);
      for (int r = 0; r < 64; ++r) {
        const unsigned* qp = (const unsigned*)(q + qo + r * 384);
        float s[8];
#pragma unroll
        for (int i = 0; i < 8; ++i) s[i] = 0.f;
        for (int d = 0; d < 96; ++d) {
          const unsigned qq = qp[d];
          const float qa = bf2f(qq & 0xffff), qb = bf2f(qq >> 16);
#pragma unroll
          for (int i = 0; i < 8; ++i) {
            const unsigned kk = kp[(i * 64 + lane) * 96 + d];
            s[i] = fmaf(qa, bf2f(kk & 0xffff), s[i]);
            s[i] = fmaf(qb, bf2f(kk >> 16), s[i]);
          }
        }
        float m = -INFINITY;
#pragma unroll
        for (int i = 0; i < 8; ++i) { s[i] *= scale; m = fmaxf(m, s[i]); }
#pragma unroll
        for (int x = 1; x < 64; x <<= 1) m = fmaxf(m, xlane(m, lane, x));
        float l = 0.f;
#pragma unroll
        for (int i = 0; i < 8; ++i) { s[i] = __expf(s[i] - m); l += s[i]; }
#pragma unroll
        for (int x = 1; x < 64; x <<= 1) l += xlane(l, lane, x);
        float a0 = 0.f, a1 = 0.f, a2 = 0.f;
#pragma unroll
        for (int i = 0; i < 8; ++i) {
          for (int x = 0; x < 64; ++x) {
            const float p = __builtin_bit_cast(float, __builtin_amdgcn_readlane(__builtin_bit_cast(int, s[i]), x));
            const unsigned short* vr = vp + (i * 64 + x) * 192 + lane;
            a0 = fmaf(p, bf2f(vr[0]), a0); a1 = fmaf(p, bf2f(vr[64]), a1); a2 = fmaf(p, bf2f(vr[128]), a2);
          }
        }
        const float il = 1.f / l;
        __hip_bfloat16* op = (__hip_bfloat16*)(o + qo + r * 384) + lane;
        op[0] = __float2bfloat16(a0 * il); op[64] = __float2bfloat16(a1 * il); op[128] = __float2bfloat16(a2 * il);
      }
    }
  }
}

extern "C" void kernel_launch(void* const* d_in, const int* in_sizes, int n_in, void* d_out, int out_size, void* d_ws, size_t ws_size,
                              hipStream_t stream) {
  attn_kernel<<<256, 256, 0, stream>>>((const char*)d_in[0], (const char*)d_in[1], (const char*)d_in[2], (char*)d_out, (char*)d_ws);
}
```
